```python
import torch

def _softmax(x, axis=-1):
    return torch.softmax(x, dim=axis)

def _gelu(x):
    return torch.nn.functional.gelu(x, approximate='tanh')

def _silu(x):
    return torch.nn.functional.silu(x)

def _sigmoid(x):
    return torch.sigmoid(x)

def _layernorm(x, axis=-1, eps=1e-05):
    m = x.mean(dim=axis, keepdim=True)
    v = x.var(dim=axis, keepdim=True, unbiased=False)
    return (x - m) / torch.sqrt(v + eps)

def _scatter_add(tgt, idx, src, axis=0):
    return tgt.clone().index_add_(axis, idx, src)

def _segment_sum(src, seg, n):
    out = torch.zeros((n,) + src.shape[1:], dtype=src.dtype)
    return out.index_add_(0, seg, src)

def _pad(x, pw):
    flat = tuple((v for pair in reversed(pw) for v in pair))
    return torch.nn.functional.pad(x, flat)
kernel_name = 'adaLN_cond__ssm_conv_h3__swiglu_ffn__prompt'

def setup_inputs():

    _w = lambda a: (lambda t: t.to(torch.bfloat16) if t.is_floating_point() else t)(torch.as_tensor(a))

    def _A_setup():
        B, S, D = (1, 2048, 448)
        ks = 4
        return {'q': _w((lambda *s: torch.randn(*s, dtype=torch.bfloat16) * 0.05)(B, S, D)), 'k': _w((lambda *s: torch.randn(*s, dtype=torch.bfloat16) * 1.6)(B, S, D)), 'v': _w((lambda *s: torch.randn(*s, dtype=torch.bfloat16) * 1.6)(B, S, D)), 'shift_k': _w((lambda *s: torch.randn(*s, dtype=torch.bfloat16) * 1.6)(D, ks) * 0.5), 'ssm_k': _w((lambda *s: torch.randn(*s, dtype=torch.bfloat16) * 1.6)(D, S) * (1.0 / S ** 0.5)), 'ssm_D': _w((lambda *s: torch.randn(*s, dtype=torch.bfloat16) * 1.6)(D))}
    _A_in = _A_setup()

    def _B_setup():
        B, S, D, F = (1, 2048, 448, 1792)
        return {'x': _w((lambda *s: torch.randn(*s, dtype=torch.bfloat16) * 0.05)(B, S, D)), 'w_gate': _w((lambda *s: torch.randn(*s, dtype=torch.bfloat16) * 0.1)(D, F)), 'w_up': _w((lambda *s: torch.randn(*s, dtype=torch.bfloat16) * 0.1)(D, F)), 'w_down': _w((lambda *s: torch.randn(*s, dtype=torch.bfloat16) * 0.1)(F, D))}
    _B_in = _B_setup()
    return {'A_q': _A_in['q'], 'A_k': _A_in['k'], 'A_v': _A_in['v'], 'A_shift_k': _A_in['shift_k'], 'A_ssm_k': _A_in['ssm_k'], 'A_ssm_D': _A_in['ssm_D'], 'B_x': _B_in['x'], 'B_w_gate': _B_in['w_gate'], 'B_w_up': _B_in['w_up'], 'B_w_down': _B_in['w_down'], 'c': _w((lambda *s: torch.randn(*s, dtype=torch.bfloat16) * 0.2)(1, 448) * 8.0), 'w_mod': _w((lambda *s: torch.randn(*s, dtype=torch.bfloat16) * 0.2)(448, 6 * 448) * 448 ** (-0.5) * 8.0)}

def reference(A_q, A_k, A_v, A_shift_k, A_ssm_k, A_ssm_D, B_x, B_w_gate, B_w_up, B_w_down, c, w_mod):

    def _A_ref(q, k, v, shift_k, ssm_k, ssm_D):
        B, S, D = q.shape
        ks = shift_k.shape[-1]
        k_pad = _pad(k, ((0, 0), (ks - 1, 0), (0, 0)))
        k_shift = torch.zeros_like(k)
        for t in range(ks):
            k_shift = k_shift + k_pad[:, t:t + S, :] * shift_k[:, ks - 1 - t][None, None, :]
        kv = k_shift * v
        L = 2 * S
        kv_pad = _pad(kv, ((0, 0), (0, S), (0, 0)))
        kf_pad = _pad(ssm_k, ((0, 0), (0, S)))
        kv_f = (lambda x, axes=(-2, -1): torch.fft.rfft2(x.float(), dim=axes))(kv_pad[:, None, :, :], axes=(1, 2))
        kf_f = (lambda x, axes=(-2, -1): torch.fft.rfft2(x.float(), dim=axes))(kf_pad[:, None, :], axes=(1, 2))
        kf_f = (lambda x, *d: x.permute(*d))(kf_f, 1, 2, 0)
        y_f = kv_f * kf_f[None, :, :, :]
        y = (lambda x, s, axes=(-2, -1): torch.fft.irfft2(x, s=s, dim=axes).to(torch.bfloat16))(y_f, (1, L), axes=(1, 2))[:, 0, :S, :]
        y = y + kv * ssm_D[None, None, :]
        return q * y

    def _B_ref(x, w_gate, w_up, w_down):
        g = _silu(x @ w_gate)
        u = x @ w_up
        return g * u @ w_down
    x = A_q
    mod = (_silu(c) @ w_mod).reshape(c.shape[0], 6, 1, -1)
    sa, ca, ga, sb, cb, gb = (mod[:, 0].clone(), mod[:, 1].clone(), mod[:, 2].clone(), mod[:, 3].clone(), mod[:, 4].clone(), mod[:, 5].clone())
    h = _layernorm(x, axis=-1) * (1 + ca) + sa
    x = x + ga * _A_ref(h, A_k, A_v, A_shift_k, A_ssm_k, A_ssm_D)
    h = _layernorm(x, axis=-1) * (1 + cb) + sb
    x = x + gb * _B_ref(h, B_w_gate, B_w_up, B_w_down)
    return x
```

```cpp
#include <hip/hip_runtime.h>
#include <hip/hip_bf16.h>
#include <stdint.h>

typedef unsigned short u16;
constexpr int S = 2048, D = 448, F = 1792;

__device__ __forceinline__ float bf2f(u16 b) { return __uint_as_float(((unsigned)b) << 16); }
__device__ __forceinline__ u16 f2bf(float f) { __hip_bfloat16 h = __float2bfloat16(f); return *reinterpret_cast<u16*>(&h); }
__device__ __forceinline__ float rbf(float f) { return bf2f(f2bf(f)); }

__global__ void k_mod(const u16* c, const u16* wmod, float* mod) {
    int n = blockIdx.x * blockDim.x + threadIdx.x;
    if (n >= 6 * D) return;
    float acc = 0.f;
    for (int k = 0; k < D; ++k) {
        float cv = bf2f(c[k]);
        float sc = rbf(cv / (1.f + __expf(-cv)));
        acc += sc * bf2f(wmod[(size_t)k * 6 * D + n]);
    }
    mod[n] = rbf(acc);
}

__global__ void k_kv(const u16* k, const u16* v, const u16* shk, float* kvT) {
    int idx = blockIdx.x * blockDim.x + threadIdx.x;
    if (idx >= S * D) return;
    int s = idx / D, d = idx % D;
    float acc = 0.f;
    for (int t = 0; t < 4; ++t) {
        int ss = s + t - 3;
        float kk = ss >= 0 ? bf2f(k[(size_t)ss * D + d]) : 0.f;
        acc = rbf(acc + rbf(kk * bf2f(shk[d * 4 + 3 - t])));
    }
    kvT[(size_t)d * S + s] = rbf(acc * bf2f(v[idx]));
}

__global__ void k_conv(const float* kvT, const u16* ssmk, const u16* ssmD, float* z) {
    __shared__ float su[S], sk[S];
    int d = blockIdx.x;
    for (int i = threadIdx.x; i < S; i += blockDim.x) { su[i] = kvT[(size_t)d * S + i]; sk[i] = bf2f(ssmk[(size_t)d * S + i]); }
    __syncthreads();
    float Dd = bf2f(ssmD[d]);
    for (int s = threadIdx.x; s < S; s += blockDim.x) {
        float acc = 0.f;
        for (int j = 0; j <= s; ++j) acc += sk[j] * su[s - j];
        float y = rbf(acc);
        z[(size_t)s * D + d] = rbf(y + rbf(su[s] * Dd));
    }
}

__global__ void k_row(const u16* x, const float* z, const float* mod, float* x1o, u16* h2o) {
    int s = blockIdx.x, lane = threadIdx.x;
    float xv[7], sum = 0.f;
    for (int i = 0; i < 7; ++i) { xv[i] = bf2f(x[(size_t)s * D + lane + 64 * i]); sum += xv[i]; }
    for (int o = 32; o; o >>= 1) sum += __shfl_xor(sum, o);
    float m = sum / D, var = 0.f;
    for (int i = 0; i < 7; ++i) var += (xv[i] - m) * (xv[i] - m);
    for (int o = 32; o; o >>= 1) var += __shfl_xor(var, o);
    float r = rsqrtf(var / D + 1e-5f);
    float x1[7]; sum = 0.f;
    for (int i = 0; i < 7; ++i) {
        int d = lane + 64 * i;
        float h = (xv[i] - m) * r * (1.f + mod[D + d]) + mod[d];
        x1[i] = rbf(xv[i] + mod[2 * D + d] * h * z[(size_t)s * D + d]);
        sum += x1[i];
    }
    for (int o = 32; o; o >>= 1) sum += __shfl_xor(sum, o);
    m = sum / D; var = 0.f;
    for (int i = 0; i < 7; ++i) var += (x1[i] - m) * (x1[i] - m);
    for (int o = 32; o; o >>= 1) var += __shfl_xor(var, o);
    r = rsqrtf(var / D + 1e-5f);
    for (int i = 0; i < 7; ++i) {
        int d = lane + 64 * i;
        x1o[(size_t)s * D + d] = x1[i];
        h2o[(size_t)s * D + d] = f2bf((x1[i] - m) * r * (1.f + mod[4 * D + d]) + mod[3 * D + d]);
    }
}

__global__ void k_g(const u16* h2, const u16* wg, const u16* wu, u16* G) {
    __shared__ float sh[D];
    int s = blockIdx.x;
    for (int i = threadIdx.x; i < D; i += blockDim.x) sh[i] = bf2f(h2[(size_t)s * D + i]);
    __syncthreads();
    for (int f = threadIdx.x; f < F; f += blockDim.x) {
        float g = 0.f, u = 0.f;
        for (int k = 0; k < D; ++k) { g += sh[k] * bf2f(wg[(size_t)k * F + f]); u += sh[k] * bf2f(wu[(size_t)k * F + f]); }
        g = rbf(g); u = rbf(u);
        float sg = rbf(g / (1.f + __expf(-g)));
        G[(size_t)s * F + f] = f2bf(sg * u);
    }
}

__global__ void k_down(const u16* G, const u16* wd, const float* x1, const float* mod, u16* out) {
    __shared__ float sg[F];
    int s = blockIdx.x;
    for (int i = threadIdx.x; i < F; i += blockDim.x) sg[i] = bf2f(G[(size_t)s * F + i]);
    __syncthreads();
    for (int d = threadIdx.x; d < D; d += blockDim.x) {
        float acc = 0.f;
        for (int k = 0; k < F; ++k) acc += sg[k] * bf2f(wd[(size_t)k * D + d]);
        out[(size_t)s * D + d] = f2bf(x1[(size_t)s * D + d] + mod[5 * D + d] * rbf(acc));
    }
}

extern "C" void kernel_launch(void* const* d_in, const int* in_sizes, int n_in,
                              void* d_out, int out_size, void* d_ws, size_t ws_size,
                              hipStream_t stream) {
    const u16* q = (const u16*)d_in[0];
    const u16* k = (const u16*)d_in[1];
    const u16* v = (const u16*)d_in[2];
    const u16* shk = (const u16*)d_in[3];
    const u16* ssmk = (const u16*)d_in[4];
    const u16* ssmD = (const u16*)d_in[5];
    const u16* wg = (const u16*)d_in[7];
    const u16* wu = (const u16*)d_in[8];
    const u16* wd = (const u16*)d_in[9];
    const u16* c = (const u16*)d_in[10];
    const u16* wmod = (const u16*)d_in[11];
    char* ws = (char*)d_ws;
    float* mod = (float*)ws;
    float* kvT = (float*)(ws + (1 << 20));
    float* z = (float*)(ws + (8 << 20));
    float* x1 = (float*)(ws + (16 << 20));
    u16* h2 = (u16*)(ws + (24 << 20));
    u16* G = (u16*)(ws + (32 << 20));
    k_mod<<<(6 * D + 63) / 64, 64, 0, stream>>>(c, wmod, mod);
    k_kv<<<(S * D + 255) / 256, 256, 0, stream>>>(k, v, shk, kvT);
    k_conv<<<D, 256, 0, stream>>>(kvT, ssmk, ssmD, z);
    k_row<<<S, 64, 0, stream>>>(q, z, mod, x1, h2);
    k_g<<<S, 256, 0, stream>>>(h2, wg, wu, G);
    k_down<<<S, 256, 0, stream>>>(G, wd, x1, mod, (u16*)d_out);
}
```

```cpp
#include <hip/hip_runtime.h>
#include <hip/hip_bf16.h>
#include <stdint.h>

typedef unsigned short u16;
constexpr int S = 2048, D = 448, F = 1792;

__device__ __forceinline__ float bf2f(u16 b) { return __uint_as_float(((unsigned)b) << 16); }
__device__ __forceinline__ u16 f2bf(float f) { __hip_bfloat16 h = __float2bfloat16(f); return *reinterpret_cast<u16*>(&h); }
__device__ __forceinline__ float rbf(float f) { return bf2f(f2bf(f)); }

__global__ void k_mod(const u16* c, const u16* wmod, float* mod) {
    int n = blockIdx.x * blockDim.x + threadIdx.x;
    if (n >= 6 * D) return;
    float acc = 0.f;
    for (int k = 0; k < D; ++k) {
        float cv = bf2f(c[k]);
        float sc = rbf(cv / (1.f + __expf(-cv)));
        acc += sc * bf2f(wmod[(size_t)k * 6 * D + n]);
    }
    mod[n] = rbf(acc);
}

__global__ void k_kv(const u16* k, const u16* v, const u16* shk, float* kvT) {
    int idx = blockIdx.x * blockDim.x + threadIdx.x;
    if (idx >= S * D) return;
    int s = idx / D, d = idx % D;
    float acc = 0.f;
    for (int t = 0; t < 4; ++t) {
        int ss = s + t - 3;
        float kk = ss >= 0 ? bf2f(k[(size_t)ss * D + d]) : 0.f;
        acc = rbf(acc + rbf(kk * bf2f(shk[d * 4 + 3 - t])));
    }
    kvT[(size_t)d * S + s] = rbf(acc * bf2f(v[idx]));
}

__global__ void k_conv(const float* kvT, const u16* ssmk, const u16* ssmD, float* z) {
    __shared__ float su[S], sk[S];
    int d = blockIdx.x;
    for (int i = threadIdx.x; i < S; i += blockDim.x) { su[i] = kvT[(size_t)d * S + i]; sk[i] = bf2f(ssmk[(size_t)d * S + i]); }
    __syncthreads();
    float Dd = bf2f(ssmD[d]);
    for (int s = threadIdx.x; s < S; s += blockDim.x) {
        float acc = 0.f;
        for (int j = 0; j <= s; ++j) acc += sk[j] * su[s - j];
        float y = rbf(acc);
        z[(size_t)s * D + d] = rbf(y + rbf(su[s] * Dd));
    }
}

__global__ void k_row(const u16* x, const float* z, const float* mod, float* x1o, u16* h2o) {
    int s = blockIdx.x, lane = threadIdx.x;
    float xv[7], sum = 0.f;
    for (int i = 0; i < 7; ++i) { xv[i] = bf2f(x[(size_t)s * D + lane + 64 * i]); sum += xv[i]; }
    for (int o = 32; o; o >>= 1) sum += __shfl_xor(sum, o);
    float m = sum / D, var = 0.f;
    for (int i = 0; i < 7; ++i) var += (xv[i] - m) * (xv[i] - m);
    for (int o = 32; o; o >>= 1) var += __shfl_xor(var, o);
    float r = rsqrtf(var / D + 1e-5f);
    float x1[7]; sum = 0.f;
    for (int i = 0; i < 7; ++i) {
        int d = lane + 64 * i;
        float h = (xv[i] - m) * r * (1.f + mod[D + d]) + mod[d];
        x1[i] = rbf(xv[i] + mod[2 * D + d] * h * z[(size_t)s * D + d]);
        sum += x1[i];
    }
    for (int o = 32; o; o >>= 1) sum += __shfl_xor(sum, o);
    m = sum / D; var = 0.f;
    for (int i = 0; i < 7; ++i) var += (x1[i] - m) * (x1[i] - m);
    for (int o = 32; o; o >>= 1) var += __shfl_xor(var, o);
    r = rsqrtf(var / D + 1e-5f);
    for (int i = 0; i < 7; ++i) {
        int d = lane + 64 * i;
        x1o[(size_t)s * D + d] = x1[i];
        h2o[(size_t)s * D + d] = f2bf((x1[i] - m) * r * (1.f + mod[4 * D + d]) + mod[3 * D + d]);
    }
}

__global__ void k_g(const u16* h2, const u16* wg, const u16* wu, u16* G) {
    __shared__ float sh[D];
    int s = blockIdx.x;
    for (int i = threadIdx.x; i < D; i += blockDim.x) sh[i] = bf2f(h2[(size_t)s * D + i]);
    __syncthreads();
    for (int f = threadIdx.x; f < F; f += blockDim.x) {
        float g = 0.f, u = 0.f;
        for (int k = 0; k < D; ++k) { g += sh[k] * bf2f(wg[(size_t)k * F + f]); u += sh[k] * bf2f(wu[(size_t)k * F + f]); }
        g = rbf(g); u = rbf(u);
        float sg = rbf(g / (1.f + __expf(-g)));
        G[(size_t)s * F + f] = f2bf(sg * u);
    }
}

__global__ void k_down(const u16* G, const u16* wd, const float* x1, const float* mod, u16* out) {
    __shared__ float sg[F];
    int s = blockIdx.x;
    for (int i = threadIdx.x; i < F; i += blockDim.x) sg[i] = bf2f(G[(size_t)s * F + i]);
    __syncthreads();
    for (int d = threadIdx.x; d < D; d += blockDim.x) {
        float acc = 0.f;
        for (int k = 0; k < F; ++k) acc += sg[k] * bf2f(wd[(size_t)k * D + d]);
        out[(size_t)s * D + d] = f2bf(x1[(size_t)s * D + d] + mod[5 * D + d] * rbf(acc));
    }
}


__global__ void k_probe(const unsigned* p, unsigned* sink, int n, int stride_dw) {
    unsigned idx = 0, acc = 0;
    for (int i = 0; i < n; ++i) { unsigned x = __builtin_nontemporal_load(p + idx); acc += x; idx += stride_dw + (x & 0u) ; asm volatile("" : "+v"(idx) : "v"(x)); }
    sink[threadIdx.x] = acc;
}
extern "C" void kernel_launch(void* const* d_in, const int* in_sizes, int n_in,
                              void* d_out, int out_size, void* d_ws, size_t ws_size,
                              hipStream_t stream) {
    const u16* q = (const u16*)d_in[0];
    const u16* k = (const u16*)d_in[1];
    const u16* v = (const u16*)d_in[2];
    const u16* shk = (const u16*)d_in[3];
    const u16* ssmk = (const u16*)d_in[4];
    const u16* ssmD = (const u16*)d_in[5];
    const u16* wg = (const u16*)d_in[7];
    const u16* wu = (const u16*)d_in[8];
    const u16* wd = (const u16*)d_in[9];
    const u16* c = (const u16*)d_in[10];
    const u16* wmod = (const u16*)d_in[11];
    char* ws = (char*)d_ws;
    float* mod = (float*)ws;
    float* kvT = (float*)(ws + (1 << 20));
    float* z = (float*)(ws + (8 << 20));
    float* x1 = (float*)(ws + (16 << 20));
    u16* h2 = (u16*)(ws + (24 << 20));
    u16* G = (u16*)(ws + (32 << 20));
    k_probe<<<1, 1, 0, stream>>>((const unsigned*)(ws + (128<<20)), (unsigned*)(ws + (64<<20)), 1000, 384);
    k_mod<<<(6 * D + 63) / 64, 64, 0, stream>>>(c, wmod, mod);
    k_kv<<<(S * D + 255) / 256, 256, 0, stream>>>(k, v, shk, kvT);
    k_conv<<<D, 256, 0, stream>>>(kvT, ssmk, ssmD, z);
    k_row<<<S, 64, 0, stream>>>(q, z, mod, x1, h2);
    k_g<<<S, 256, 0, stream>>>(h2, wg, wu, G);
    k_down<<<S, 256, 0, stream>>>(G, wd, x1, mod, (u16*)d_out);
}
```

```cpp
#include <hip/hip_runtime.h>
#include <hip/hip_bf16.h>
#include <stdint.h>

typedef unsigned short u16;
constexpr int S = 2048, D = 448, F = 1792;

__device__ __forceinline__ float bf2f(u16 b) { return __uint_as_float(((unsigned)b) << 16); }
__device__ __forceinline__ u16 f2bf(float f) { __hip_bfloat16 h = __float2bfloat16(f); return *reinterpret_cast<u16*>(&h); }
__device__ __forceinline__ float rbf(float f) { return f; }
__device__ __forceinline__ float rbf2(float f) { return bf2f(f2bf(f)); }

__global__ void k_mod(const u16* c, const u16* wmod, float* mod) {
    int n = blockIdx.x * blockDim.x + threadIdx.x;
    if (n >= 6 * D) return;
    float acc = 0.f;
    for (int k = 0; k < D; ++k) {
        float cv = bf2f(c[k]);
        float sc = rbf(cv / (1.f + __expf(-cv)));
        acc += sc * bf2f(wmod[(size_t)k * 6 * D + n]);
    }
    mod[n] = rbf(acc);
}

__global__ void k_kv(const u16* k, const u16* v, const u16* shk, float* kvT) {
    int idx = blockIdx.x * blockDim.x + threadIdx.x;
    if (idx >= S * D) return;
    int s = idx / D, d = idx % D;
    float acc = 0.f;
    for (int t = 0; t < 4; ++t) {
        int ss = s + t - 3;
        float kk = ss >= 0 ? bf2f(k[(size_t)ss * D + d]) : 0.f;
        acc = rbf(acc + rbf(kk * bf2f(shk[d * 4 + 3 - t])));
    }
    kvT[(size_t)d * S + s] = rbf2(acc * bf2f(v[idx]));
}

__global__ void k_conv(const float* kvT, const u16* ssmk, const u16* ssmD, float* z) {
    __shared__ float su[S], sk[S];
    int d = blockIdx.x;
    for (int i = threadIdx.x; i < S; i += blockDim.x) { su[i] = kvT[(size_t)d * S + i]; sk[i] = bf2f(ssmk[(size_t)d * S + i]); }
    __syncthreads();
    float Dd = bf2f(ssmD[d]);
    for (int s = threadIdx.x; s < S; s += blockDim.x) {
        float acc = 0.f;
        for (int j = 0; j <= s; ++j) acc += sk[j] * su[s - j];
        float y = rbf(acc);
        z[(size_t)s * D + d] = rbf(y + rbf(su[s] * Dd));
    }
}

__global__ void k_row(const u16* x, const float* z, const float* mod, float* x1o, u16* h2o) {
    int s = blockIdx.x, lane = threadIdx.x;
    float xv[7], sum = 0.f;
    for (int i = 0; i < 7; ++i) { xv[i] = bf2f(x[(size_t)s * D + lane + 64 * i]); sum += xv[i]; }
    for (int o = 32; o; o >>= 1) sum += __shfl_xor(sum, o);
    float m = sum / D, var = 0.f;
    for (int i = 0; i < 7; ++i) var += (xv[i] - m) * (xv[i] - m);
    for (int o = 32; o; o >>= 1) var += __shfl_xor(var, o);
    float r = rsqrtf(var / D + 1e-5f);
    float x1[7]; sum = 0.f;
    for (int i = 0; i < 7; ++i) {
        int d = lane + 64 * i;
        float h = (xv[i] - m) * r * (1.f + mod[D + d]) + mod[d];
        x1[i] = rbf(xv[i] + mod[2 * D + d] * h * z[(size_t)s * D + d]);
        sum += x1[i];
    }
    for (int o = 32; o; o >>= 1) sum += __shfl_xor(sum, o);
    m = sum / D; var = 0.f;
    for (int i = 0; i < 7; ++i) var += (x1[i] - m) * (x1[i] - m);
    for (int o = 32; o; o >>= 1) var += __shfl_xor(var, o);
    r = rsqrtf(var / D + 1e-5f);
    for (int i = 0; i < 7; ++i) {
        int d = lane + 64 * i;
        x1o[(size_t)s * D + d] = x1[i];
        h2o[(size_t)s * D + d] = f2bf((x1[i] - m) * r * (1.f + mod[4 * D + d]) + mod[3 * D + d]);
    }
}

__global__ void k_g(const u16* h2, const u16* wg, const u16* wu, u16* G) {
    __shared__ float sh[D];
    int s = blockIdx.x;
    for (int i = threadIdx.x; i < D; i += blockDim.x) sh[i] = bf2f(h2[(size_t)s * D + i]);
    __syncthreads();
    for (int f = threadIdx.x; f < F; f += blockDim.x) {
        float g = 0.f, u = 0.f;
        for (int k = 0; k < D; ++k) { g += sh[k] * bf2f(wg[(size_t)k * F + f]); u += sh[k] * bf2f(wu[(size_t)k * F + f]); }
        g = rbf(g); u = rbf(u);
        float sg = rbf(g / (1.f + __expf(-g)));
        G[(size_t)s * F + f] = f2bf(sg * u);
    }
}

__global__ void k_down(const u16* G, const u16* wd, const float* x1, const float* mod, u16* out) {
    __shared__ float sg[F];
    int s = blockIdx.x;
    for (int i = threadIdx.x; i < F; i += blockDim.x) sg[i] = bf2f(G[(size_t)s * F + i]);
    __syncthreads();
    for (int d = threadIdx.x; d < D; d += blockDim.x) {
        float acc = 0.f;
        for (int k = 0; k < F; ++k) acc += sg[k] * bf2f(wd[(size_t)k * D + d]);
        out[(size_t)s * D + d] = f2bf(x1[(size_t)s * D + d] + mod[5 * D + d] * rbf(acc));
    }
}

extern "C" void kernel_launch(void* const* d_in, const int* in_sizes, int n_in,
                              void* d_out, int out_size, void* d_ws, size_t ws_size,
                              hipStream_t stream) {
    const u16* q = (const u16*)d_in[0];
    const u16* k = (const u16*)d_in[1];
    const u16* v = (const u16*)d_in[2];
    const u16* shk = (const u16*)d_in[3];
    const u16* ssmk = (const u16*)d_in[4];
    const u16* ssmD = (const u16*)d_in[5];
    const u16* wg = (const u16*)d_in[7];
    const u16* wu = (const u16*)d_in[8];
    const u16* wd = (const u16*)d_in[9];
    const u16* c = (const u16*)d_in[10];
    const u16* wmod = (const u16*)d_in[11];
    char* ws = (char*)d_ws;
    float* mod = (float*)ws;
    float* kvT = (float*)(ws + (1 << 20));
    float* z = (float*)(ws + (8 << 20));
    float* x1 = (float*)(ws + (16 << 20));
    u16* h2 = (u16*)(ws + (24 << 20));
    u16* G = (u16*)(ws + (32 << 20));
    k_mod<<<(6 * D + 63) / 64, 64, 0, stream>>>(c, wmod, mod);
    k_kv<<<(S * D + 255) / 256, 256, 0, stream>>>(k, v, shk, kvT);
    k_conv<<<D, 256, 0, stream>>>(kvT, ssmk, ssmD, z);
    k_row<<<S, 64, 0, stream>>>(q, z, mod, x1, h2);
    k_g<<<S, 256, 0, stream>>>(h2, wg, wu, G);
    k_down<<<S, 256, 0, stream>>>(G, wd, x1, mod, (u16*)d_out);
}
```

```cpp
#include <hip/hip_runtime.h>
#include <hip/hip_bf16.h>
#include <stdint.h>
#define USE_NT

typedef unsigned short u16;
typedef unsigned long long u64;
typedef unsigned v4u __attribute__((ext_vector_type(4)));
typedef unsigned v2u __attribute__((ext_vector_type(2)));
typedef float v4f __attribute__((ext_vector_type(4)));
typedef float v2f __attribute__((ext_vector_type(2)));
typedef float v16f __attribute__((ext_vector_type(16)));
typedef short v8s __attribute__((ext_vector_type(8)));
typedef short v4s __attribute__((ext_vector_type(4)));
typedef __bf16 v2bf __attribute__((ext_vector_type(2)));
typedef __bf16 v8bf __attribute__((ext_vector_type(8)));
typedef v4u __attribute__((aligned(2))) v4u_a2;
#define LAS __attribute__((address_space(3)))
#define GAS __attribute__((address_space(1)))

constexpr int S = 2048, D = 448, F = 1792, NP = 224;
constexpr int NWORK = 224, NHELP = 32;

constexpr int O_EPOCH = 0;
constexpr int O_TMO = 1024;
constexpr int O_CC = 4096;
constexpr int O_ST1 = 12288;
constexpr int O_ST2 = 20480;
constexpr int O_MODP = 36864;
constexpr int O_FX = 81920;
constexpr int O_FA = 114688;
constexpr int O_CB = 172032;
constexpr int O_KVP = 229376;
constexpr int O_XP = O_KVP + NP * S * 4;
constexpr int O_AP = O_XP + NP * S * 4;
constexpr int O_X1P = O_AP + NP * S * 4;
constexpr int O_G = O_X1P + NP * S * 4;
constexpr int O_END = O_G + S * F * 2;
constexpr int O_STAMP = 32 << 20;

constexpr int L_W1 = 0;
constexpr int L_U = 114688;
constexpr int UST = 4608;
constexpr int L_EX = L_U + 2 * UST;
constexpr int L_KL = L_EX + 16384;
constexpr int KST = 4160;
constexpr int L_UV = L_EX + 32768;
constexpr int L_CS = L_UV + 1024;
constexpr int L_GB = L_CS + 1792;
constexpr int L_MISC = L_GB + 128;
constexpr int L_TOTAL = L_MISC + 64;
static_assert(L_TOTAL <= 163840, "lds");
#define CNT(i) (L + L_MISC + 4 * (i))

constexpr int SC1 = 16;
constexpr int SPIN_MAX = 1 << 18;

struct Params {
    const u16 *q, *k, *v, *shk, *ssmk, *ssmD, *wg, *wu, *wd, *c, *wmod;
    u16* out;
    char* ws;
};

__device__ __forceinline__ float lo2f(unsigned x) { return __uint_as_float(x << 16); }
__device__ __forceinline__ float hi2f(unsigned x) { return __uint_as_float(x & 0xffff0000u); }
__device__ __forceinline__ unsigned pk(float a, float b) {
    v2f x = {a, b};
    return __builtin_bit_cast(unsigned, __builtin_convertvector(x, v2bf));
}
__device__ __forceinline__ float rbf(float a) { return lo2f(pk(a, a)); }
__device__ __forceinline__ void rb2(float& a, float& b) { unsigned p = pk(a, b); a = lo2f(p); b = hi2f(p); }

#define LDIN(p) __builtin_nontemporal_load(p)
#define BAR() asm volatile("s_waitcnt lgkmcnt(0)\n\ts_barrier" ::: "memory")
#define DRAIN() asm volatile("s_waitcnt vmcnt(0)" ::: "memory")

#define STAMP(i)
#define STAMP_DECL
#define STAMP_OUT

__device__ __forceinline__ v4f mfma16(v4u a, v4u b, v4f c) {
    return __builtin_amdgcn_mfma_f32_16x16x32_bf16(__builtin_bit_cast(v8bf, a), __builtin_bit_cast(v8bf, b), c, 0, 0, 0);
}
__device__ __forceinline__ v16f mfma32(v4u a, v4u b, v16f c) {
    return __builtin_amdgcn_mfma_f32_32x32x16_bf16(__builtin_bit_cast(v8bf, a), __builtin_bit_cast(v8bf, b), c, 0, 0, 0);
}

typedef __amdgpu_buffer_rsrc_t rsrc_t;
__device__ __forceinline__ v4u ld128(rsrc_t r, int off) { return __builtin_amdgcn_raw_buffer_load_b128(r, off, 0, SC1); }
__device__ __forceinline__ v2u ld64(rsrc_t r, int off) { return __builtin_amdgcn_raw_buffer_load_b64(r, off, 0, SC1); }
__device__ __forceinline__ unsigned ld32(rsrc_t r, int off) { return __builtin_amdgcn_raw_buffer_load_b32(r, off, 0, SC1); }
__device__ __forceinline__ void st128(rsrc_t r, int off, v4u v) { __builtin_amdgcn_raw_buffer_store_b128(v, r, off, 0, SC1); }
__device__ __forceinline__ void st64(rsrc_t r, int off, v2u v) { __builtin_amdgcn_raw_buffer_store_b64(v, r, off, 0, SC1); }

__device__ __forceinline__ void wait_words(rsrc_t r, int off, u64 want, char* ws, unsigned code) {
#pragma unroll 1
    for (int spins = 0;; ++spins) {
        asm volatile("" ::: "memory");
        v2u x = ld64(r, off);
        bool ok = x.x == (unsigned)want && x.y == (unsigned)(want >> 32);
        if (__all(ok)) break;
        if (spins > SPIN_MAX) { __builtin_amdgcn_raw_buffer_store_b32(code, r, O_TMO, 0, SC1); break; }
    }
    __builtin_amdgcn_fence(__ATOMIC_ACQUIRE, "wavefront");
    asm volatile("" ::: "memory");
}
__device__ __forceinline__ void add_word(char* ws, int off) {
    __hip_atomic_fetch_add((u64*)(ws + off), 1ull, __ATOMIC_RELAXED, __HIP_MEMORY_SCOPE_AGENT);
}

__device__ __forceinline__ void glds16(const void* gsrc, unsigned lds_dst) {
    unsigned keep;
    asm volatile("s_mov_b32 %0, m0\n\ts_mov_b32 m0, %2\n\ts_nop 0\n\tglobal_load_lds_dwordx4 %1, off\n\ts_mov_b32 m0, %0"
                 : "=&s"(keep) : "v"(gsrc), "s"(lds_dst) : "memory");
}

__device__ __forceinline__ void lds_arrive(LAS char* p, int lane) {
    asm volatile("s_waitcnt lgkmcnt(0)" ::: "memory");
    if (lane == 0) __hip_atomic_fetch_add((LAS unsigned*)p, 1u, __ATOMIC_RELAXED, __HIP_MEMORY_SCOPE_WORKGROUP);
}
__device__ __forceinline__ void lds_wait(LAS char* p, unsigned n) {
#pragma unroll 1
    while (__hip_atomic_load((LAS unsigned*)p, __ATOMIC_RELAXED, __HIP_MEMORY_SCOPE_WORKGROUP) < n) __builtin_amdgcn_s_sleep(1);
    asm volatile("" ::: "memory");
}
#define NW1A 56
#define WD1 6
#define WDS 2
#define WARM_WD 0
#define AUXIN 2
__device__ __forceinline__ float rdl(float v, int l) { return __uint_as_float(__builtin_amdgcn_readlane(__float_as_uint(v), l)); }
__device__ __forceinline__ float silu_f(float g) { return g * __builtin_amdgcn_rcpf(1.f + __expf(-g)); }

__global__ __launch_bounds__(512, 2) void fused(Params p) {
    extern __shared__ __attribute__((aligned(16))) char smem[];
    LAS char* L = (LAS char*)smem;
    const int b = blockIdx.x, tid = threadIdx.x, lane = tid & 63;
    const int w = __builtin_amdgcn_readfirstlane(tid >> 6);
    const rsrc_t rs = __builtin_amdgcn_make_buffer_rsrc(p.ws, 0, O_END, 0x00020000);
    STAMP_DECL;
    STAMP(0);
    u64 epoch, base;
#define GET_EPOCH() do { asm volatile("s_load_dwordx2 %0, %1, 0x0\n\ts_waitcnt lgkmcnt(0)" : "=s"(epoch) : "s"(p.ws) : "memory"); base = (epoch << 16) + 0x9E3779B97F4A0000ull; } while (0)
#define basev (v2u{(unsigned)base, (unsigned)(base >> 32)})
    const int x = b & 7;

    const unsigned tag32 = 0;
    (void)tag32;
#define TAG32 ((unsigned)(base >> 16) ^ 0x5bd1e995u)
    auto stats1 = [&](const v4u (&xr)[7]) {
        float sum = 0.f;
#pragma unroll
        for (int i = 0; i < 7; ++i)
#pragma unroll
            for (int j = 0; j < 4; ++j) sum += lo2f(xr[i][j]) + hi2f(xr[i][j]);
        sum += __shfl_xor(sum, 1); sum += __shfl_xor(sum, 2); sum += __shfl_xor(sum, 4);
        const float m = sum * (1.f / D);
        float var = 0.f;
#pragma unroll
        for (int i = 0; i < 7; ++i)
#pragma unroll
            for (int j = 0; j < 4; ++j) { float a = lo2f(xr[i][j]) - m, c = hi2f(xr[i][j]) - m; var = fmaf(a, a, var); var = fmaf(c, c, var); }
        var += __shfl_xor(var, 1); var += __shfl_xor(var, 2); var += __shfl_xor(var, 4);
        const float sd = __builtin_amdgcn_sqrtf(rbf(rbf(var * (1.f / D)) + 1e-5f));
        if ((lane & 7) == 0) __builtin_amdgcn_raw_buffer_store_b32(pk(m, sd), rs, O_ST1 + (8 * b + (lane >> 3)) * 4, 0, SC1);
    };
    const rsrc_t rq = __builtin_amdgcn_make_buffer_rsrc((void*)p.q, 0, S * D * 2, 0x00020000);

    if (b < NWORK) {
        const int g = b >> 5, rt = b & 31, idx = b >> 3;
        const int nt1 = 7 * (x >> 1) + idx % 7, pn1 = 4 * (x & 1) + idx / 7;
        const int rt2 = idx / 7, nt2 = idx % 7;
        const int cl = w >> 2, i0b = 2 * (w & 3);
        if (w < 4) {
            unsigned kk[11], vv[8];
            const int pp = tid & 31, rg = tid >> 5;
            const int row0 = 64 * rt + 8 * rg, col = 32 * g + pp;
            const rsrc_t rk = __builtin_amdgcn_make_buffer_rsrc((void*)p.k, 0, S * D * 2, 0x00020000);
            const rsrc_t rv = __builtin_amdgcn_make_buffer_rsrc((void*)p.v, 0, S * D * 2, 0x00020000);
            const int o0 = (row0 * NP + col) * 4;
            const int oh = row0 ? o0 - 3 * 896 : 0x40000000;
#pragma unroll
            for (int i = 0; i < 3; ++i) kk[i] = __builtin_amdgcn_raw_buffer_load_b32(rk, oh + i * 896, 0, AUXIN);
#pragma unroll
            for (int i = 0; i < 8; ++i) kk[3 + i] = __builtin_amdgcn_raw_buffer_load_b32(rk, o0 + i * 896, 0, AUXIN);
#pragma unroll
            for (int i = 0; i < 8; ++i) vv[i] = __builtin_amdgcn_raw_buffer_load_b32(rv, o0 + i * 896, 0, AUXIN);
            const v4u sh = *(const v4u*)(p.shk + col * 8);
            const v4u kf0 = LDIN((const v4u*)(p.ssmk + (size_t)2 * b * S + tid * 8)), kf1 = LDIN((const v4u*)(p.ssmk + (size_t)(2 * b + 1) * S + tid * 8));
            __builtin_amdgcn_sched_barrier(0);
            asm volatile("s_barrier" ::: "memory");
            GET_EPOCH();
            __builtin_amdgcn_sched_barrier(0);
            float s0[4] = {lo2f(sh.x), hi2f(sh.x), lo2f(sh.y), hi2f(sh.y)};
            float s1[4] = {lo2f(sh.z), hi2f(sh.z), lo2f(sh.w), hi2f(sh.w)};
            unsigned o[8];
#pragma unroll
            for (int i = 0; i < 8; ++i) {
                float a0, a1;
                a0 = lo2f(kk[i]) * s0[3]; a1 = hi2f(kk[i]) * s1[3];
#pragma unroll
                for (int t = 1; t < 4; ++t) { a0 = fmaf(lo2f(kk[i + t]), s0[3 - t], a0); a1 = fmaf(hi2f(kk[i + t]), s1[3 - t], a1); }
                o[i] = pk(a0 * lo2f(vv[i]), a1 * hi2f(vv[i]));
            }
            const int la = L_EX + pp * 256;
            const int sw0 = ((2 * rg) ^ (pp & 15)) * 16;
            *(LAS v4u*)(L + la + sw0) = v4u{o[0], o[1], o[2], o[3]};
            *(LAS v4u*)(L + la + (sw0 ^ 16)) = v4u{o[4], o[5], o[6], o[7]};
            lds_arrive(CNT(0), lane);
            lds_wait(CNT(0), 4);
            STAMP(1);
#pragma unroll
            for (int i = 0; i < 2; ++i) {
                const int p2 = 8 * w + 4 * i + (lane >> 4), sl = lane & 15;
                v4u a = *(LAS v4u*)(L + L_EX + p2 * 256 + ((sl ^ (p2 & 15)) * 16));
                st128(rs, O_KVP + (32 * g + p2) * (S * 4) + (64 * rt + 4 * sl) * 4, a);
            }
            if (rt == 0 && lane < 32) st64(rs, O_CB + ((lane * 7 + g) * 4 + w) * 64, basev);
            if (b == 0 && w == 0) st64(rs, O_CC + lane * 128, basev);
            if (w == 2 && lane < 10 && b * 10 + lane < S) st64(rs, O_ST2 + (b * 10 + lane) * 8, v2u{0u, ~TAG32});
            *(LAS v4u*)(L + L_KL + 32 + tid * 16) = kf0;
            *(LAS v4u*)(L + L_KL + KST + 32 + tid * 16) = kf1;
            lds_arrive(CNT(1), lane);
            DRAIN();
            if (lane == 0) st64(rs, O_FA + (w * NWORK + b) * 64, basev);
            STAMP(2);
        } else if (w < 6) {
            unsigned xx[16];
            const int pp = tid & 31, rg = (tid >> 5) & 3;
            const int o0 = ((64 * rt + 16 * rg) * NP + 32 * g + pp) * 4;
#pragma unroll
            for (int i = 0; i < 16; ++i) xx[i] = __builtin_amdgcn_raw_buffer_load_b32(rq, o0 + i * 896, 0, 0);
            v4u xr[14]; unsigned cv[14];
            const int slab = b >> 2, ks = b & 3;
            if (w == 4) {
#pragma unroll
                for (int i = 0; i < 7; ++i) xr[i] = __builtin_amdgcn_raw_buffer_load_b128(rq, (8 * b + (lane >> 3)) * 896 + ((lane & 7) + 8 * i) * 16, 0, 0);
                *(LAS v4u*)(L + L_U + (lane >> 5) * UST + 4096 + (lane & 31) * 16) = v4u{0, 0, 0, 0};
                if (lane < 4) *(LAS v4u*)(L + L_KL + (lane >> 1) * KST + (lane & 1) * 16) = v4u{0, 0, 0, 0};
                if (lane < 4) *(LAS v4u*)(L + L_MISC + lane * 16) = v4u{0, 0, 0, 0};
            } else {
                const rsrc_t rm = __builtin_amdgcn_make_buffer_rsrc((void*)p.wmod, 0, b < 168 ? D * 6 * D * 2 : 0, 0x00020000);
                const rsrc_t rc = __builtin_amdgcn_make_buffer_rsrc((void*)p.c, 0, D * 2, 0x00020000);
#pragma unroll
                for (int i = 0; i < 14; ++i) {
                    xr[i] = __builtin_amdgcn_raw_buffer_load_b128(rm, (112 * ks + 8 * i + (lane >> 3)) * (6 * D * 2) + slab * 128 + (lane & 7) * 16, 0, AUXIN);
                    cv[i] = __builtin_amdgcn_raw_buffer_load_b16(rc, (112 * ks + 8 * i + (lane >> 3)) * 2, 0, 0);
                }
            }
            __builtin_amdgcn_sched_barrier(0);
            BAR();
            GET_EPOCH();
            __builtin_amdgcn_sched_barrier(0);
#pragma unroll
            for (int i = 0; i < 4; ++i)
                *(LAS v4u*)(L + L_EX + 8192 + pp * 256 + (((4 * rg + i) ^ (pp & 15)) * 16)) = v4u{xx[4 * i], xx[4 * i + 1], xx[4 * i + 2], xx[4 * i + 3]};
            lds_arrive(CNT(2), lane);
            if (w == 4) {
                stats1((const v4u(&)[7])xr);
            } else {
                float a[8];
#pragma unroll
                for (int j = 0; j < 8; ++j) a[j] = 0.f;
#pragma unroll
                for (int i = 0; i < 14; ++i) {
                    const float cf = lo2f(cv[i]);
                    const float sc = rbf(cf * __builtin_amdgcn_rcpf(1.f + __expf(-cf)));
#pragma unroll
                    for (int j = 0; j < 4; ++j) { a[2 * j] = fmaf(sc, lo2f(xr[i][j]), a[2 * j]); a[2 * j + 1] = fmaf(sc, hi2f(xr[i][j]), a[2 * j + 1]); }
                }
                const bool b5 = lane & 32, b4 = lane & 16, b3 = lane & 8;
                float c4[4], c2[2];
#pragma unroll
                for (int j = 0; j < 4; ++j) { float keep = b5 ? a[4 + j] : a[j], send = b5 ? a[j] : a[4 + j]; c4[j] = keep + __shfl_xor(send, 32); }
#pragma unroll
                for (int j = 0; j < 2; ++j) { float keep = b4 ? c4[2 + j] : c4[j], send = b4 ? c4[j] : c4[2 + j]; c2[j] = keep + __shfl_xor(send, 16); }
                const float c1 = (b3 ? c2[1] : c2[0]) + __shfl_xor(b3 ? c2[0] : c2[1], 8);
                const int colm = slab * 64 + (lane & 7) * 8 + (lane >> 3);
                if (b < 168) __builtin_amdgcn_raw_buffer_store_b32(__float_as_uint(c1), rs, O_MODP + colm * 16 + ks * 4, 0, SC1);
            }
            lds_wait(CNT(2), 2);
            STAMP(1);
#pragma unroll
            for (int i = 0; i < 4; ++i) {
                const int p2 = 16 * (w - 4) + 4 * i + (lane >> 4), sl = lane & 15;
                v4u a = *(LAS v4u*)(L + L_EX + 8192 + p2 * 256 + ((sl ^ (p2 & 15)) * 16));
                st128(rs, O_XP + (32 * g + p2) * (S * 4) + (64 * rt + 4 * sl) * 4, a);
            }
            DRAIN();
            if (lane == 0) st64(rs, O_FX + ((w - 4) * 256 + b) * 64, basev);
            STAMP(2);
        } else {
            asm volatile("s_barrier" ::: "memory");
            const int r8 = lane >> 3, ch = lane & 7;
            const int mat = w - 6;
            const u16* wsrc = (mat ? p.wu : p.wg) + (size_t)r8 * F + nt1 * 64 + ((ch ^ (((r8 >> 1) & 1) << 2)) * 8);
            int i = (idx / 7) * 14;
#pragma unroll 2
            for (int n = 0; n < NW1A; ++n) {
                glds16(wsrc + (size_t)i * 8 * F, (unsigned)(size_t)(L + L_W1 + (i >> 1) * 4096 + mat * 2048 + (i & 1) * 1024));
                i = i == 55 ? 0 : i + 1;
            }
            STAMP(1);
            GET_EPOCH();
        }
        const int la_ = lane & 15, q_ = lane >> 4;
        lds_wait(CNT(1), 4);
        v4u KF[2][8];
        {
            const int kb = L_KL + cl * KST + ((2 + 2 * la_ + 16 * q_) & ~3) + 64 * i0b;
            const int shf = (la_ & 1) ? 0 : 16;
#pragma unroll
            for (int ii = 0; ii < 2; ++ii)
#pragma unroll
                for (int sg = 0; sg < 8; ++sg) {
                    unsigned d[5];
#pragma unroll
                    for (int j = 0; j < 5; ++j) d[j] = *(LAS unsigned*)(L + kb + ii * 64 + sg * 512 + j * 4);
#pragma unroll
                    for (int j = 0; j < 4; ++j) KF[ii][sg][j] = __builtin_amdgcn_alignbit(d[j + 1], d[j], shf);
                    asm volatile("" : "+v"(KF[ii][sg]));
                }
        }
        STAMP(3);
        if (w == 0) wait_words(rs, O_FA + (((b >> 3) & 3) * NWORK + 32 * g + (lane & 31)) * 64, base, p.ws, 1);
        BAR();
        STAMP(4);
        const int s0 = 256 * w + 16 * la_ + 4 * q_;
        int fxo;
        {
            int f;
            if (lane < 32) f = 32 * w + lane;
            else if (lane < 36) f = ((b >> 4) & 1) * 256 + 32 * g + 4 * w + (lane - 32);
            else if (lane < 52) { const int vi = (lane - 36) >> 2; f = 256 + (((vi == 3 ? 4 : vi) * 7 + g) * 4 + (lane & 3)); }
            else { const int sbx = w < 7 ? ((lane & 4) ? 21 + w : 28 + w) : 35 + nt2; f = 256 + sbx * 4 + (lane & 3); }
            fxo = O_FX + f * 64;
        }
        v4u kv4;
        if (w < 4) {
            kv4 = ld128(rs, O_KVP + b * (S * 4) + s0 * 4);
            v4u kvb = ld128(rs, O_KVP + b * (S * 4) + (s0 + 1024) * 4);
            const int ua = L_U + (2044 - s0) * 2;
            *(LAS v2u*)(L + ua) = v2u{__builtin_amdgcn_perm(kv4.z, kv4.w, 0x05040100), __builtin_amdgcn_perm(kv4.x, kv4.y, 0x05040100)};
            *(LAS v2u*)(L + ua + UST) = v2u{__builtin_amdgcn_perm(kv4.z, kv4.w, 0x07060302), __builtin_amdgcn_perm(kv4.x, kv4.y, 0x07060302)};
            *(LAS v2u*)(L + ua - 2048) = v2u{__builtin_amdgcn_perm(kvb.z, kvb.w, 0x05040100), __builtin_amdgcn_perm(kvb.x, kvb.y, 0x05040100)};
            *(LAS v2u*)(L + ua - 2048 + UST) = v2u{__builtin_amdgcn_perm(kvb.z, kvb.w, 0x07060302), __builtin_amdgcn_perm(kvb.x, kvb.y, 0x07060302)};
        }
        wait_words(rs, fxo, base, p.ws, 2);
        const v4u st1 = ld128(rs, O_ST1 + s0 * 4);
        const v4u x4 = ld128(rs, O_XP + b * (S * 4) + s0 * 4);
        if (w >= 4) kv4 = ld128(rs, O_KVP + b * (S * 4) + s0 * 4);
        const int vi_ = (lane >> 1) & 3;
        const v4u mp = ld128(rs, O_MODP + ((vi_ == 3 ? 4 : vi_) * D + 2 * b + (lane & 1)) * 16);
        const v4u cs0 = ld128(rs, O_MODP + ((w < 7 ? 21 + w : 35 + nt2) * 64 + lane) * 16);
        const v4u cs1 = ld128(rs, O_MODP + ((28 + w) * 64 + lane) * 16);
        const unsigned m_D = ((const unsigned*)p.ssmD)[b];
        __builtin_amdgcn_sched_barrier(0);
        BAR();
        STAMP(5);
        if (NW1A < 56 && w >= 6) {
            const int r8 = lane >> 3, ch = lane & 7;
            const int mat = w - 6;
            const u16* wsrc = (mat ? p.wu : p.wg) + (size_t)r8 * F + nt1 * 64 + ((ch ^ (((r8 >> 1) & 1) << 2)) * 8);
            int i = ((idx / 7) * 14 + NW1A) % 56;
#pragma unroll 2
            for (int n = NW1A; n < 56; ++n) {
                glds16(wsrc + (size_t)i * 8 * F, (unsigned)(size_t)(L + L_W1 + (i >> 1) * 4096 + mat * 2048 + (i & 1) * 1024));
                i = i == 55 ? 0 : i + 1;
            }
        }
        const int c31 = lane & 31, h_ = lane >> 5;
        unsigned WD[14][8];
        const rsrc_t rwd = __builtin_amdgcn_make_buffer_rsrc((void*)p.wd, 0, F * D * 2, 0x00020000);
        const int wdo = ((224 * w + 4 * h_) * NP + nt2 * 32 + c31) * 4;
#define LDWD(kk2, j) __builtin_amdgcn_raw_buffer_load_b32(rwd, wdo + ((j) & 3) * 896 + (16 * (kk2) + 8 * ((j) >> 2)) * 896, 0, 0)
#pragma unroll
        for (int kk2 = 0; kk2 < WD1; ++kk2)
#pragma unroll
            for (int j = 0; j < 8; ++j) WD[kk2][j] = LDWD(kk2, j);
        __builtin_amdgcn_sched_barrier(0);
        STAMP(6);
        v4f acc[8];
#pragma unroll
        for (int t = 0; t < 8; ++t) acc[t] = v4f{0.f, 0.f, 0.f, 0.f};
        const int ub = L_U + cl * UST + (2032 - 16 * la_ + 8 * q_ + 32 * i0b) * 2;
        v4u UF[2][4];
#pragma unroll
        for (int ii = 0; ii < 2; ++ii)
#pragma unroll
            for (int r = 0; r < 4; ++r) UF[ii][r] = *(LAS v4u*)(L + ub + ii * 64 - r * 512);
        __builtin_amdgcn_sched_barrier(0);
#pragma unroll
        for (int ii = 0; ii < 2; ++ii)
#pragma unroll
            for (int r = 0; r < 4; ++r)
#pragma unroll
                for (int sg = 0; sg + r < 4; ++sg) acc[sg + r] = mfma16(KF[ii][sg], UF[ii][r], acc[sg + r]);
#pragma unroll
        for (int t = 0; t < 4; ++t) *(LAS v4f*)(L + L_EX + (w * 4 + t) * 1024 + lane * 16) = acc[t];
        BAR();
        STAMP(7);
        auto finalize = [&](int tl) {
            v4f y0 = {0.f, 0.f, 0.f, 0.f}, y1 = {0.f, 0.f, 0.f, 0.f};
#pragma unroll
            for (int ww = 0; ww < 4; ++ww) {
                y0 += *(LAS v4f*)(L + L_EX + (ww * 4 + tl) * 1024 + lane * 16);
                y1 += *(LAS v4f*)(L + L_EX + ((ww + 4) * 4 + tl) * 1024 + lane * 16);
            }
            float mv = rbf((__uint_as_float(mp.x) + __uint_as_float(mp.y)) + (__uint_as_float(mp.z) + __uint_as_float(mp.w)));
            if (vi_ & 1) mv = rbf(1.f + mv);
            const float sa0 = rdl(mv, 0), sa1 = rdl(mv, 1), ca0 = rdl(mv, 2), ca1 = rdl(mv, 3), ga0 = rdl(mv, 4), ga1 = rdl(mv, 5), cb0 = rdl(mv, 6), cb1 = rdl(mv, 7);
            unsigned oa[4], ox[4];
#pragma unroll
            for (int i = 0; i < 4; ++i) {
                float a0 = y0[i], a1 = y1[i];
                const float xa = lo2f(x4[i]), xb = hi2f(x4[i]);
                const float mean = lo2f(st1[i]), rstd = __builtin_amdgcn_rcpf(hi2f(st1[i]));
                a0 = fmaf(lo2f(kv4[i]), lo2f(m_D), a0); a1 = fmaf(hi2f(kv4[i]), hi2f(m_D), a1);
                float n0 = (xa - mean) * rstd, n1 = (xb - mean) * rstd;
                n0 = fmaf(n0, ca0, sa0); n1 = fmaf(n1, ca1, sa1);
                ox[i] = pk(fmaf(n0 * a0, ga0, xa), fmaf(n1 * a1, ga1, xb));
                oa[i] = pk(lo2f(ox[i]) * cb0, hi2f(ox[i]) * cb1);
            }
            const int go = ((8 * w + (la_ >> 1)) * NP + b) * 128 + (16 * (la_ & 1) + 4 * q_) * 4;
            st128(rs, O_AP + go, v4u{oa[0], oa[1], oa[2], oa[3]});
            st128(rs, O_X1P + go, v4u{ox[0], ox[1], ox[2], ox[3]});
        };
        auto publishB = [&]() {
            DRAIN();
            if (lane < 4) add_word(p.ws, O_CB + (((w * 4 + lane) * 7 + g) * 4 + ((b >> 3) & 3)) * 64);
        };
        if (w < 4) finalize(w);
        __builtin_amdgcn_sched_barrier(0);
        STAMP(8);
#pragma unroll
        for (int ii = 0; ii < 2; ++ii)
#pragma unroll
            for (int r = 0; r < 4; ++r)
#pragma unroll
                for (int sg = 4 - r; sg + r < 8; ++sg) acc[sg + r] = mfma16(KF[ii][sg], UF[ii][r], acc[sg + r]);
        __builtin_amdgcn_sched_barrier(0);
        if (w < 4) publishB();
        __builtin_amdgcn_sched_barrier(0);
#pragma unroll
        for (int ii = 0; ii < 2; ++ii)
#pragma unroll
            for (int r = 0; r < 4; ++r) UF[ii][r] = *(LAS v4u*)(L + ub + ii * 64 - (r + 4) * 512);
        __builtin_amdgcn_sched_barrier(0);
#pragma unroll
        for (int ii = 0; ii < 2; ++ii)
#pragma unroll
            for (int r = 0; r < 4; ++r)
#pragma unroll
                for (int sg = 0; sg + r < 4; ++sg) acc[4 + sg + r] = mfma16(KF[ii][sg], UF[ii][r], acc[4 + sg + r]);
        {
            float a = rbf((__uint_as_float(cs0.x) + __uint_as_float(cs0.y)) + (__uint_as_float(cs0.z) + __uint_as_float(cs0.w)));
            float c = 1.f + rbf((__uint_as_float(cs1.x) + __uint_as_float(cs1.y)) + (__uint_as_float(cs1.z) + __uint_as_float(cs1.w)));
            const unsigned ac = pk(a, c);
            *(LAS u16*)(L + (w < 7 ? L_CS + w * 128 : L_GB) + lane * 2) = (u16)ac;
            if (w < 7) *(LAS u16*)(L + L_CS + 896 + w * 128 + lane * 2) = (u16)(ac >> 16);
        }
        BAR();
#pragma unroll
        for (int t = 0; t < 4; ++t) *(LAS v4f*)(L + L_EX + (w * 4 + t) * 1024 + lane * 16) = acc[4 + t];
        if (w >= 6) DRAIN();
        BAR();
        STAMP(9);
        const int tr_e = (lane & 15) >> 2, tr_p = lane & 3, tr_ch = (lane >> 4) & 1;
        const int trb = L_W1 + (8 * h_ + tr_e) * 128 + 32 * tr_ch + 8 * tr_p;
        const int trx = ((tr_e >> 1) & 1) * 64;
        auto wfrag = [&](int tile, int k1) -> v4u {
            const int o = trb + k1 * 4096 + (tile >> 1) * 2048 + (((tile & 1) * 64) ^ trx);
            v4s a = __builtin_amdgcn_ds_read_tr16_b64_v4i16((LAS v4s*)(L + o));
            v4s c = __builtin_amdgcn_ds_read_tr16_b64_v4i16((LAS v4s*)(L + o + 512));
            v2u a2 = __builtin_bit_cast(v2u, a), c2 = __builtin_bit_cast(v2u, c);
            return v4u{a2.x, a2.y, c2.x, c2.y};
        };
        if (w >= 4) { finalize(w - 4); lds_arrive(CNT(11), lane); publishB(); }
        else {
            v16f uacc;
#pragma unroll
            for (int i = 0; i < 16; ++i) uacc[i] = 0.f;
            const int mo = L_CS + (c31 < 16 ? 896 : 0) + 16 * h_;
#pragma unroll 1
            for (int k0 = 0; k0 < 28; k0 += 7) {
                v4u wf[7], xf[7];
#pragma unroll
                for (int i = 0; i < 7; ++i) { wf[i] = wfrag(w, k0 + i); xf[i] = *(LAS v4u*)(L + mo + (k0 + i) * 32); }
                __builtin_amdgcn_sched_barrier(0);
#pragma unroll
                for (int i = 0; i < 7; ++i) uacc = mfma32(wf[i], xf[i], uacc);
            }
            if ((lane & 15) == 0) {
#pragma unroll
                for (int g4 = 0; g4 < 4; ++g4)
                    *(LAS v4f*)(L + L_UV + ((c31 >> 4) * 128 + w * 32 + 8 * g4 + 4 * h_) * 4) = v4f{uacc[4 * g4], uacc[4 * g4 + 1], uacc[4 * g4 + 2], uacc[4 * g4 + 3]};
            }
            lds_arrive(CNT(5), lane);
        }
        STAMP(10);
        const int sb1 = cl * 32 + 4 * pn1 + (w & 3);
        if ((w & 3) == 0) {
            const int l28 = lane < 28 ? lane : 0;
            wait_words(rs, O_CB + (((cl * 4 + (pn1 >> 1)) * 4 + (x >> 1)) * 28 + l28) * 64, base + 8, p.ws, 3);
            lds_arrive(CNT(3 + cl), lane);
        } else lds_wait(CNT(3 + cl), 1);
        STAMP(11);
        v16f ga[4];
#pragma unroll
        for (int t = 0; t < 4; ++t)
#pragma unroll
            for (int i = 0; i < 16; ++i) ga[t][i] = 0.f;
        v2u st2;
        {
            constexpr int WIN = 8;
            v4u XF[WIN];
            v4u WA[2][4];
            const int xo = O_AP + sb1 * (NP * 128) + h_ * 512 + c31 * 4;
            auto ldx = [&](int k1) -> v4u {
                return v4u{ld32(rs, xo + k1 * 1024), ld32(rs, xo + k1 * 1024 + 128), ld32(rs, xo + k1 * 1024 + 256), ld32(rs, xo + k1 * 1024 + 384)};
            };
#pragma unroll
            for (int i = 0; i < WIN; ++i) XF[i] = ldx(i);
#pragma unroll
            for (int t = 0; t < 4; ++t) WA[0][t] = wfrag(t, 0);
            __builtin_amdgcn_sched_barrier(0);
#pragma unroll
            for (int k1 = 0; k1 < 28; ++k1) {
                if (k1 + 1 < 28) {
#pragma unroll
                    for (int t = 0; t < 4; ++t) WA[(k1 + 1) & 1][t] = wfrag(t, k1 + 1);
                }
                __builtin_amdgcn_sched_barrier(0);
                const v4u xf = XF[k1 % WIN];
#pragma unroll
                for (int t = 0; t < 4; ++t) ga[t] = mfma32(WA[k1 & 1][t], xf, ga[t]);
                __builtin_amdgcn_sched_barrier(0);
                if (k1 == 0) STAMP(12);
                if (k1 + WIN < 28) XF[k1 % WIN] = ldx(k1 + WIN);
                if (k1 >= WDS && k1 - WDS < 2 * (14 - WD1)) {
                    const int kk2 = WD1 + ((k1 - WDS) >> 1), jh = ((k1 - WDS) & 1) * 4;
#pragma unroll
                    for (int j = 0; j < 4; ++j) WD[kk2][jh + j] = LDWD(kk2, jh + j);
                }
                if (k1 == 24) st2 = ld64(rs, O_ST2 + (sb1 * 32 + c31) * 8);
                __builtin_amdgcn_sched_barrier(0);
            }
        }
        lds_arrive(CNT(8), lane);
        STAMP(13);
        {
#pragma unroll 1
            for (int spins = 0; !__all(st2.y == TAG32); ++spins) {
                asm volatile("" ::: "memory");
                st2 = ld64(rs, O_ST2 + (sb1 * 32 + c31) * 8);
                if (spins > SPIN_MAX) { __builtin_amdgcn_raw_buffer_store_b32(4, rs, O_TMO, 0, SC1); break; }
            }
            lds_wait(CNT(5), 4);
            STAMP(14);
            const float r2 = __builtin_amdgcn_rcpf(hi2f(st2.x)), nmr = -lo2f(st2.x) * r2;
#pragma unroll
            for (int t = 0; t < 2; ++t) {
                unsigned o[8];
#pragma unroll
                for (int g4 = 0; g4 < 4; ++g4) {
                    const int uo = L_UV + (t * 32 + 8 * g4 + 4 * h_) * 4;
                    v4f u1g = *(LAS v4f*)(L + uo), u1u = *(LAS v4f*)(L + uo + 256), u2g = *(LAS v4f*)(L + uo + 512), u2u = *(LAS v4f*)(L + uo + 768);
                    float r[4];
#pragma unroll
                    for (int i = 0; i < 4; ++i) {
                        float gg = fmaf(r2, ga[t][4 * g4 + i], fmaf(nmr, u1g[i], u2g[i]));
                        float uu = fmaf(r2, ga[2 + t][4 * g4 + i], fmaf(nmr, u1u[i], u2u[i]));
                        r[i] = silu_f(gg) * uu;
                    }
                    o[2 * g4] = pk(r[0], r[1]); o[2 * g4 + 1] = pk(r[2], r[3]);
                }
                const int go = O_G + (sb1 * 112 + 4 * nt1 + 2 * t) * 1024 + lane * 16;
                st128(rs, go, v4u{o[0], o[1], o[2], o[3]});
                st128(rs, go + 1024, v4u{o[4], o[5], o[6], o[7]});
            }
            STAMP(15);
            DRAIN();
            if (lane == 0) add_word(p.ws, O_CC + sb1 * 128);
        }
        STAMP(16);
        v4u WF[14][2];
#pragma unroll
        for (int kk2 = 0; kk2 < 14; ++kk2) {
#pragma unroll
            for (int i = 0; i < 4; ++i) {
                WF[kk2][0][i] = __builtin_amdgcn_perm(WD[kk2][2 * i + 1], WD[kk2][2 * i], 0x05040100);
                WF[kk2][1][i] = __builtin_amdgcn_perm(WD[kk2][2 * i + 1], WD[kk2][2 * i], 0x07060302);
            }
            asm volatile("" : "+v"(WF[kk2][0]), "+v"(WF[kk2][1]));
        }
#pragma unroll
        for (int st = 0; st < 2; ++st) {
            const int sb2 = st * 32 + 4 * x + rt2;
            if (w == 4 * st) { wait_words(rs, O_CC + sb2 * 128, base + 28, p.ws, 5); lds_arrive(CNT(6 + st), lane); }
            else lds_wait(CNT(6 + st), 1);
            STAMP(17 + 3 * st);
            v4u GF[14];
            const int go = O_G + (sb2 * 112 + 14 * w) * 1024 + lane * 16;
#pragma unroll
            for (int i = 0; i < 14; ++i) GF[i] = ld128(rs, go + i * 1024);
            unsigned x1v[4];
            if (st == 0) {
#pragma unroll
                for (int i = 0; i < 4; ++i) x1v[i] = ld32(rs, O_X1P + (sb2 * NP + 32 * nt2 + 4 * (tid & 7) + i) * 128 + ((tid >> 3) & 31) * 4);
            } else {
#pragma unroll
                for (int i = 0; i < 2; ++i) x1v[i] = ld32(rs, O_X1P + (sb2 * NP + 32 * nt2 + 2 * (tid & 15) + i) * 128 + (tid >> 4) * 4);
            }
            __builtin_amdgcn_sched_barrier(0);
            v16f oa[2];
#pragma unroll
            for (int i = 0; i < 16; ++i) { oa[0][i] = 0.f; oa[1][i] = 0.f; }
#pragma unroll
            for (int kk2 = 0; kk2 < 14; ++kk2) {
                oa[0] = mfma32(WF[kk2][0], GF[kk2], oa[0]);
                oa[1] = mfma32(WF[kk2][1], GF[kk2], oa[1]);
            }
            STAMP(18 + 3 * st);
            const int po = st ? 32768 + w * 8192 : (w < 4 ? L_EX + w * 8192 : (w - 4) * 8192);
            if (st == 0 && w < 4) lds_wait(CNT(11), 4); else lds_wait(CNT(8), 8);
#pragma unroll
            for (int t = 0; t < 2; ++t)
#pragma unroll
                for (int g4 = 0; g4 < 4; ++g4)
                    *(LAS v4f*)(L + po + t * 4096 + c31 * 128 + (((2 * g4 + h_) ^ (c31 & 7)) * 16)) =
                        v4f{oa[t][4 * g4], oa[t][4 * g4 + 1], oa[t][4 * g4 + 2], oa[t][4 * g4 + 3]};
            lds_arrive(CNT(9 + st), lane);
            if (st == 0) {
                if (w < 4) {
                    const int sl = tid >> 3, cg = tid & 7;
                    lds_wait(CNT(9), 8);
                    v4f r0 = {0.f, 0.f, 0.f, 0.f}, r1 = {0.f, 0.f, 0.f, 0.f};
                    const int ro = sl * 128 + ((cg ^ (sl & 7)) * 16);
#pragma unroll
                    for (int ww = 0; ww < 4; ++ww) {
                        r0 += *(LAS v4f*)(L + L_EX + ww * 8192 + ro); r1 += *(LAS v4f*)(L + L_EX + ww * 8192 + 4096 + ro);
                        r0 += *(LAS v4f*)(L + ww * 8192 + ro); r1 += *(LAS v4f*)(L + ww * 8192 + 4096 + ro);
                    }
                    const v4u gbv = *(LAS v4u*)(L + L_GB + cg * 16);
                    unsigned o[4];
#pragma unroll
                    for (int i = 0; i < 4; ++i) {
                        float a0 = r0[i], a1 = r1[i]; rb2(a0, a1);
                        a0 *= lo2f(gbv[i]); a1 *= hi2f(gbv[i]); rb2(a0, a1);
                        o[i] = pk(lo2f(x1v[i]) + a0, hi2f(x1v[i]) + a1);
                    }
                    *(v4u*)(p.out + (size_t)(sb2 * 32 + sl) * D + 64 * nt2 + 8 * cg) = v4u{o[0], o[1], o[2], o[3]};
                }
            } else {
                const int sl = tid >> 4, cg = (tid >> 1) & 7, hf = tid & 1;
                lds_wait(CNT(10), 8);
                v2f r0 = {0.f, 0.f}, r1 = {0.f, 0.f};
                const int ro = 32768 + sl * 128 + ((cg ^ (sl & 7)) * 16) + hf * 8;
#pragma unroll
                for (int ww = 0; ww < 8; ++ww) { r0 += *(LAS v2f*)(L + ww * 8192 + ro); r1 += *(LAS v2f*)(L + ww * 8192 + 4096 + ro); }
                const v2u gbv = *(LAS v2u*)(L + L_GB + (tid & 15) * 8);
                unsigned o[2];
#pragma unroll
                for (int i = 0; i < 2; ++i) {
                    float a0 = r0[i], a1 = r1[i]; rb2(a0, a1);
                    a0 *= lo2f(gbv[i]); a1 *= hi2f(gbv[i]); rb2(a0, a1);
                    o[i] = pk(lo2f(x1v[i]) + a0, hi2f(x1v[i]) + a1);
                }
                *(v2u*)(p.out + (size_t)(sb2 * 32 + sl) * D + 64 * nt2 + 4 * (tid & 15)) = v2u{o[0], o[1]};
            }
            STAMP(19 + 3 * st);
        }
    } else {
        const int hid = b - NWORK, hi = hid >> 3;
        if (w == 4) {
            v4u xr[7];
#pragma unroll
            for (int i = 0; i < 7; ++i) xr[i] = __builtin_amdgcn_raw_buffer_load_b128(rq, (8 * b + (lane >> 3)) * 896 + ((lane & 7) + 8 * i) * 16, 0, 0);
            if (lane < 4) *(LAS v4u*)(L + L_MISC + lane * 16) = v4u{0, 0, 0, 0};
            __builtin_amdgcn_sched_barrier(0);
            GET_EPOCH();
            if (lane == 0) *(LAS v2u*)(L + 65536) = v2u{(unsigned)epoch, (unsigned)(epoch >> 32)};
            stats1(xr);
            DRAIN();
            if (lane == 0) st64(rs, O_FX + b * 64, basev);
            STAMP(2);
        } else {
        }
        BAR();
        {
            const v2u e2 = *(LAS v2u*)(L + 65536);
            epoch = (u64)(unsigned)__builtin_amdgcn_readfirstlane(e2.x) | ((u64)(unsigned)__builtin_amdgcn_readfirstlane(e2.y) << 32);
            base = (epoch << 16) + 0x9E3779B97F4A0000ull;
        }
        const int st = w >> 2, t4 = tid & 255;
        const int sb = st * 32 + 4 * x + hi;
        if ((w & 3) == 0) {
            const int l28 = lane < 28 ? lane : 0;
            wait_words(rs, O_CB + (((sb >> 3) * 4 + (x >> 1)) * 28 + l28) * 64, base + 8, p.ws, 6);
            lds_arrive(CNT(st), lane);
        } else lds_wait(CNT(st), 1);
        STAMP(11);
        const int sq = t4 & 7, pg = t4 >> 3;
        const int xo = O_X1P + sb * (NP * 128) + sq * 16;
        v4u xv[7];
#pragma unroll
        for (int i = 0; i < 7; ++i) xv[i] = ld128(rs, xo + (pg + 32 * i) * 128);
        const v4u pv = ld128(rs, xo);
        float s1[4], s2[4];
#pragma unroll
        for (int j = 0; j < 4; ++j) {
            const float piv = lo2f(pv[j]);
            s1[j] = 0.f; s2[j] = 0.f;
#pragma unroll
            for (int i = 0; i < 7; ++i) {
                float a = lo2f(xv[i][j]) - piv, c = hi2f(xv[i][j]) - piv;
                s1[j] += a + c; s2[j] = fmaf(a, a, fmaf(c, c, s2[j]));
            }
        }
        const int lo_ = st * 16384;
        *(LAS v4f*)(L + lo_ + pg * 144 + sq * 16) = v4f{s1[0], s1[1], s1[2], s1[3]};
        *(LAS v4f*)(L + lo_ + 4608 + pg * 144 + sq * 16) = v4f{s2[0], s2[1], s2[2], s2[3]};
        if (t4 < 8) *(LAS v4f*)(L + lo_ + 9216 + sq * 16) = v4f{lo2f(pv[0]), lo2f(pv[1]), lo2f(pv[2]), lo2f(pv[3])};
        lds_arrive(CNT(2 + st), lane);
        lds_wait(CNT(2 + st), 4);
        {
            const int rr = (w & 3) * 8 + (lane & 7), part = (lane >> 3) & 3, arr = lane >> 5;
            float a = 0.f;
#pragma unroll
            for (int i = 0; i < 8; ++i) a += *(LAS float*)(L + lo_ + arr * 4608 + (part * 8 + i) * 144 + rr * 4);
            a += __shfl_xor(a, 8); a += __shfl_xor(a, 16);
            const float c = __shfl_xor(a, 32);
            const float piv = *(LAS float*)(L + lo_ + 9216 + rr * 4);
            const float md = a * (1.f / D);
            const float var = fmaxf(c * (1.f / D) - md * md, 0.f);
            const float sd = __builtin_amdgcn_sqrtf(rbf(rbf(var) + 1e-5f));
            if (lane < 8) st64(rs, O_ST2 + (sb * 32 + rr) * 8, v2u{pk(piv + md, sd), TAG32});
        }
        STAMP(12);
        if (hid == 7 && tid == 256) {
            const u64 e = epoch + 1;
            st64(rs, O_EPOCH, v2u{(unsigned)e, (unsigned)(e >> 32)});
        }
    }
    STAMP_OUT;
}

extern "C" void kernel_launch(void* const* d_in, const int* in_sizes, int n_in,
                              void* d_out, int out_size, void* d_ws, size_t ws_size,
                              hipStream_t stream) {
    Params p;
    p.q = (const u16*)d_in[0]; p.k = (const u16*)d_in[1]; p.v = (const u16*)d_in[2];
    p.shk = (const u16*)d_in[3]; p.ssmk = (const u16*)d_in[4]; p.ssmD = (const u16*)d_in[5];
    p.wg = (const u16*)d_in[7]; p.wu = (const u16*)d_in[8]; p.wd = (const u16*)d_in[9];
    p.c = (const u16*)d_in[10]; p.wmod = (const u16*)d_in[11];
    p.out = (u16*)d_out; p.ws = (char*)d_ws;
    (void)hipFuncSetAttribute((const void*)fused, hipFuncAttributeMaxDynamicSharedMemorySize, L_TOTAL);
    fused<<<NWORK + NHELP, 512, L_TOTAL, stream>>>(p);
}
```

```cpp
#include <hip/hip_runtime.h>
#include <hip/hip_bf16.h>
#include <stdint.h>
#define USE_NT
#define CODE_PF 33000

typedef unsigned short u16;
typedef unsigned long long u64;
typedef unsigned v4u __attribute__((ext_vector_type(4)));
typedef unsigned v2u __attribute__((ext_vector_type(2)));
typedef float v4f __attribute__((ext_vector_type(4)));
typedef float v2f __attribute__((ext_vector_type(2)));
typedef float v16f __attribute__((ext_vector_type(16)));
typedef short v8s __attribute__((ext_vector_type(8)));
typedef short v4s __attribute__((ext_vector_type(4)));
typedef __bf16 v2bf __attribute__((ext_vector_type(2)));
typedef __bf16 v8bf __attribute__((ext_vector_type(8)));
typedef v4u __attribute__((aligned(2))) v4u_a2;
#define LAS __attribute__((address_space(3)))
#define GAS __attribute__((address_space(1)))

constexpr int S = 2048, D = 448, F = 1792, NP = 224;
constexpr int NWORK = 224, NHELP = 32;

constexpr int O_EPOCH = 0;
constexpr int O_TMO = 1024;
constexpr int O_CC = 4096;
constexpr int O_ST1 = 12288;
constexpr int O_ST2 = 20480;
constexpr int O_MODP = 36864;
constexpr int O_FX = 81920;
constexpr int O_FA = 114688;
constexpr int O_CB = 172032;
constexpr int O_KVP = 229376;
constexpr int O_XP = O_KVP + NP * S * 4;
constexpr int O_AP = O_XP + NP * S * 4;
constexpr int O_X1P = O_AP + NP * S * 4;
constexpr int O_G = O_X1P + NP * S * 4;
constexpr int O_END = O_G + S * F * 2;
constexpr int O_STAMP = 32 << 20;

constexpr int L_W1 = 0;
constexpr int L_U = 114688;
constexpr int UST = 4608;
constexpr int L_EX = L_U + 2 * UST;
constexpr int L_KL = L_EX + 16384;
constexpr int KST = 4160;
constexpr int L_UV = L_EX + 32768;
constexpr int L_CS = L_UV + 1024;
constexpr int L_GB = L_CS + 1792;
constexpr int L_MISC = L_GB + 128;
constexpr int L_JUNK = L_MISC + 64;
constexpr int L_TOTAL = L_JUNK + 1024;
static_assert(L_TOTAL <= 163840, "lds");
#define CNT(i) (L + L_MISC + 4 * (i))

constexpr int SC1 = 16;
constexpr int SPIN_MAX = 1 << 18;

struct Params {
    const u16 *q, *k, *v, *shk, *ssmk, *ssmD, *wg, *wu, *wd, *c, *wmod;
    u16* out;
    char* ws;
};

__device__ __forceinline__ float lo2f(unsigned x) { return __uint_as_float(x << 16); }
__device__ __forceinline__ float hi2f(unsigned x) { return __uint_as_float(x & 0xffff0000u); }
__device__ __forceinline__ unsigned pk(float a, float b) {
    v2f x = {a, b};
    return __builtin_bit_cast(unsigned, __builtin_convertvector(x, v2bf));
}
__device__ __forceinline__ float rbf(float a) { return lo2f(pk(a, a)); }
__device__ __forceinline__ void rb2(float& a, float& b) { unsigned p = pk(a, b); a = lo2f(p); b = hi2f(p); }

#define LDIN(p) __builtin_nontemporal_load(p)
#define BAR() asm volatile("s_waitcnt lgkmcnt(0)\n\ts_barrier" ::: "memory")
#define DRAIN() asm volatile("s_waitcnt vmcnt(0)" ::: "memory")

#define STAMP(i)
#define STAMP_DECL
#define STAMP_OUT

__device__ __forceinline__ v4f mfma16(v4u a, v4u b, v4f c) {
    return __builtin_amdgcn_mfma_f32_16x16x32_bf16(__builtin_bit_cast(v8bf, a), __builtin_bit_cast(v8bf, b), c, 0, 0, 0);
}
__device__ __forceinline__ v16f mfma32(v4u a, v4u b, v16f c) {
    return __builtin_amdgcn_mfma_f32_32x32x16_bf16(__builtin_bit_cast(v8bf, a), __builtin_bit_cast(v8bf, b), c, 0, 0, 0);
}

typedef __amdgpu_buffer_rsrc_t rsrc_t;
__device__ __forceinline__ v4u ld128(rsrc_t r, int off) { return __builtin_amdgcn_raw_buffer_load_b128(r, off, 0, SC1); }
__device__ __forceinline__ v2u ld64(rsrc_t r, int off) { return __builtin_amdgcn_raw_buffer_load_b64(r, off, 0, SC1); }
__device__ __forceinline__ unsigned ld32(rsrc_t r, int off) { return __builtin_amdgcn_raw_buffer_load_b32(r, off, 0, SC1); }
__device__ __forceinline__ void st128(rsrc_t r, int off, v4u v) { __builtin_amdgcn_raw_buffer_store_b128(v, r, off, 0, SC1); }
__device__ __forceinline__ void st64(rsrc_t r, int off, v2u v) { __builtin_amdgcn_raw_buffer_store_b64(v, r, off, 0, SC1); }

__device__ __forceinline__ void wait_words(rsrc_t r, int off, u64 want, char* ws, unsigned code) {
#pragma unroll 1
    for (int spins = 0;; ++spins) {
        asm volatile("" ::: "memory");
        v2u x = ld64(r, off);
        bool ok = x.x == (unsigned)want && x.y == (unsigned)(want >> 32);
        if (__all(ok)) break;
        if (spins > SPIN_MAX) { __builtin_amdgcn_raw_buffer_store_b32(code, r, O_TMO, 0, SC1); break; }
    }
    __builtin_amdgcn_fence(__ATOMIC_ACQUIRE, "wavefront");
    asm volatile("" ::: "memory");
}
__device__ __forceinline__ void add_word(char* ws, int off) {
    __hip_atomic_fetch_add((u64*)(ws + off), 1ull, __ATOMIC_RELAXED, __HIP_MEMORY_SCOPE_AGENT);
}

__device__ __forceinline__ void glds16(const void* gsrc, unsigned lds_dst) {
    unsigned keep;
    asm volatile("s_mov_b32 %0, m0\n\ts_mov_b32 m0, %2\n\ts_nop 0\n\tglobal_load_lds_dwordx4 %1, off\n\ts_mov_b32 m0, %0"
                 : "=&s"(keep) : "v"(gsrc), "s"(lds_dst) : "memory");
}

__device__ __forceinline__ void lds_arrive(LAS char* p, int lane) {
    asm volatile("s_waitcnt lgkmcnt(0)" ::: "memory");
    if (lane == 0) __hip_atomic_fetch_add((LAS unsigned*)p, 1u, __ATOMIC_RELAXED, __HIP_MEMORY_SCOPE_WORKGROUP);
}
__device__ __forceinline__ void lds_wait(LAS char* p, unsigned n) {
#pragma unroll 1
    while (__hip_atomic_load((LAS unsigned*)p, __ATOMIC_RELAXED, __HIP_MEMORY_SCOPE_WORKGROUP) < n) __builtin_amdgcn_s_sleep(1);
    asm volatile("" ::: "memory");
}
#define NW1A 56
#define WD1 6
#define WDS 2
#define WARM_WD 0
#define AUXIN 2
__device__ __forceinline__ float rdl(float v, int l) { return __uint_as_float(__builtin_amdgcn_readlane(__float_as_uint(v), l)); }
__device__ __forceinline__ float silu_f(float g) { return g * __builtin_amdgcn_rcpf(1.f + __expf(-g)); }

__global__ __launch_bounds__(512, 2) void fused(Params p) {
    extern __shared__ __attribute__((aligned(16))) char smem[];
    LAS char* L = (LAS char*)smem;
    const int b = blockIdx.x, tid = threadIdx.x, lane = tid & 63;
    const int w = __builtin_amdgcn_readfirstlane(tid >> 6);
    const rsrc_t rs = __builtin_amdgcn_make_buffer_rsrc(p.ws, 0, O_END, 0x00020000);
    STAMP_DECL;
    STAMP(0);
    u64 epoch, base;
#define GET_EPOCH() do { asm volatile("s_load_dwordx2 %0, %1, 0x0\n\ts_waitcnt lgkmcnt(0)" : "=s"(epoch) : "s"(p.ws) : "memory"); base = (epoch << 16) + 0x9E3779B97F4A0000ull; } while (0)
#define basev (v2u{(unsigned)base, (unsigned)(base >> 32)})
    const int x = b & 7;
    if (w == 7) {
        u64 pc;
        asm volatile("s_getpc_b64 %0" : "=s"(pc));
        const char* cb = (const char*)(pc & ~63ull);
        const int chunk = (b >> 3);
        glds16(cb + (size_t)min(chunk * 2048 + lane * 16, CODE_PF - 16), (unsigned)(size_t)(L + L_JUNK));
        glds16(cb + (size_t)min(chunk * 2048 + 1024 + lane * 16, CODE_PF - 16), (unsigned)(size_t)(L + L_JUNK));
    }

    const unsigned tag32 = 0;
    (void)tag32;
#define TAG32 ((unsigned)(base >> 16) ^ 0x5bd1e995u)
    auto stats1 = [&](const v4u (&xr)[7]) {
        float sum = 0.f;
#pragma unroll
        for (int i = 0; i < 7; ++i)
#pragma unroll
            for (int j = 0; j < 4; ++j) sum += lo2f(xr[i][j]) + hi2f(xr[i][j]);
        sum += __shfl_xor(sum, 1); sum += __shfl_xor(sum, 2); sum += __shfl_xor(sum, 4);
        const float m = sum * (1.f / D);
        float var = 0.f;
#pragma unroll
        for (int i = 0; i < 7; ++i)
#pragma unroll
            for (int j = 0; j < 4; ++j) { float a = lo2f(xr[i][j]) - m, c = hi2f(xr[i][j]) - m; var = fmaf(a, a, var); var = fmaf(c, c, var); }
        var += __shfl_xor(var, 1); var += __shfl_xor(var, 2); var += __shfl_xor(var, 4);
        const float sd = __builtin_amdgcn_sqrtf(rbf(rbf(var * (1.f / D)) + 1e-5f));
        if ((lane & 7) == 0) __builtin_amdgcn_raw_buffer_store_b32(pk(m, sd), rs, O_ST1 + (8 * b + (lane >> 3)) * 4, 0, SC1);
    };
    const rsrc_t rq = __builtin_amdgcn_make_buffer_rsrc((void*)p.q, 0, S * D * 2, 0x00020000);

    if (b < NWORK) {
        const int g = b >> 5, rt = b & 31, idx = b >> 3;
        const int nt1 = 7 * (x >> 1) + idx % 7, pn1 = 4 * (x & 1) + idx / 7;
        const int rt2 = idx / 7, nt2 = idx % 7;
        const int cl = w >> 2, i0b = 2 * (w & 3);
        if (w < 4) {
            unsigned kk[11], vv[8];
            const int pp = tid & 31, rg = tid >> 5;
            const int row0 = 64 * rt + 8 * rg, col = 32 * g + pp;
            const rsrc_t rk = __builtin_amdgcn_make_buffer_rsrc((void*)p.k, 0, S * D * 2, 0x00020000);
            const rsrc_t rv = __builtin_amdgcn_make_buffer_rsrc((void*)p.v, 0, S * D * 2, 0x00020000);
            const int o0 = (row0 * NP + col) * 4;
            const int oh = row0 ? o0 - 3 * 896 : 0x40000000;
#pragma unroll
            for (int i = 0; i < 3; ++i) kk[i] = __builtin_amdgcn_raw_buffer_load_b32(rk, oh + i * 896, 0, AUXIN);
#pragma unroll
            for (int i = 0; i < 8; ++i) kk[3 + i] = __builtin_amdgcn_raw_buffer_load_b32(rk, o0 + i * 896, 0, AUXIN);
#pragma unroll
            for (int i = 0; i < 8; ++i) vv[i] = __builtin_amdgcn_raw_buffer_load_b32(rv, o0 + i * 896, 0, AUXIN);
            const v4u sh = *(const v4u*)(p.shk + col * 8);
            const v4u kf0 = LDIN((const v4u*)(p.ssmk + (size_t)2 * b * S + tid * 8)), kf1 = LDIN((const v4u*)(p.ssmk + (size_t)(2 * b + 1) * S + tid * 8));
            __builtin_amdgcn_sched_barrier(0);
            asm volatile("s_barrier" ::: "memory");
            GET_EPOCH();
            __builtin_amdgcn_sched_barrier(0);
            float s0[4] = {lo2f(sh.x), hi2f(sh.x), lo2f(sh.y), hi2f(sh.y)};
            float s1[4] = {lo2f(sh.z), hi2f(sh.z), lo2f(sh.w), hi2f(sh.w)};
            unsigned o[8];
#pragma unroll
            for (int i = 0; i < 8; ++i) {
                float a0, a1;
                a0 = lo2f(kk[i]) * s0[3]; a1 = hi2f(kk[i]) * s1[3];
#pragma unroll
                for (int t = 1; t < 4; ++t) { a0 = fmaf(lo2f(kk[i + t]), s0[3 - t], a0); a1 = fmaf(hi2f(kk[i + t]), s1[3 - t], a1); }
                o[i] = pk(a0 * lo2f(vv[i]), a1 * hi2f(vv[i]));
            }
            const int la = L_EX + pp * 256;
            const int sw0 = ((2 * rg) ^ (pp & 15)) * 16;
            *(LAS v4u*)(L + la + sw0) = v4u{o[0], o[1], o[2], o[3]};
            *(LAS v4u*)(L + la + (sw0 ^ 16)) = v4u{o[4], o[5], o[6], o[7]};
            lds_arrive(CNT(0), lane);
            lds_wait(CNT(0), 4);
            STAMP(1);
#pragma unroll
            for (int i = 0; i < 2; ++i) {
                const int p2 = 8 * w + 4 * i + (lane >> 4), sl = lane & 15;
                v4u a = *(LAS v4u*)(L + L_EX + p2 * 256 + ((sl ^ (p2 & 15)) * 16));
                st128(rs, O_KVP + (32 * g + p2) * (S * 4) + (64 * rt + 4 * sl) * 4, a);
            }
            if (rt == 0 && lane < 32) st64(rs, O_CB + ((lane * 7 + g) * 4 + w) * 64, basev);
            if (b == 0 && w == 0) st64(rs, O_CC + lane * 128, basev);
            if (w == 2 && lane < 10 && b * 10 + lane < S) st64(rs, O_ST2 + (b * 10 + lane) * 8, v2u{0u, ~TAG32});
            *(LAS v4u*)(L + L_KL + 32 + tid * 16) = kf0;
            *(LAS v4u*)(L + L_KL + KST + 32 + tid * 16) = kf1;
            lds_arrive(CNT(1), lane);
            DRAIN();
            if (lane == 0) st64(rs, O_FA + (w * NWORK + b) * 64, basev);
            STAMP(2);
        } else if (w < 6) {
            unsigned xx[16];
            const int pp = tid & 31, rg = (tid >> 5) & 3;
            const int o0 = ((64 * rt + 16 * rg) * NP + 32 * g + pp) * 4;
#pragma unroll
            for (int i = 0; i < 16; ++i) xx[i] = __builtin_amdgcn_raw_buffer_load_b32(rq, o0 + i * 896, 0, 0);
            v4u xr[14]; unsigned cv[14];
            const int slab = b >> 2, ks = b & 3;
            if (w == 4) {
#pragma unroll
                for (int i = 0; i < 7; ++i) xr[i] = __builtin_amdgcn_raw_buffer_load_b128(rq, (8 * b + (lane >> 3)) * 896 + ((lane & 7) + 8 * i) * 16, 0, 0);
                *(LAS v4u*)(L + L_U + (lane >> 5) * UST + 4096 + (lane & 31) * 16) = v4u{0, 0, 0, 0};
                if (lane < 4) *(LAS v4u*)(L + L_KL + (lane >> 1) * KST + (lane & 1) * 16) = v4u{0, 0, 0, 0};
                if (lane < 4) *(LAS v4u*)(L + L_MISC + lane * 16) = v4u{0, 0, 0, 0};
            } else {
                const rsrc_t rm = __builtin_amdgcn_make_buffer_rsrc((void*)p.wmod, 0, b < 168 ? D * 6 * D * 2 : 0, 0x00020000);
                const rsrc_t rc = __builtin_amdgcn_make_buffer_rsrc((void*)p.c, 0, D * 2, 0x00020000);
#pragma unroll
                for (int i = 0; i < 14; ++i) {
                    xr[i] = __builtin_amdgcn_raw_buffer_load_b128(rm, (112 * ks + 8 * i + (lane >> 3)) * (6 * D * 2) + slab * 128 + (lane & 7) * 16, 0, AUXIN);
                    cv[i] = __builtin_amdgcn_raw_buffer_load_b16(rc, (112 * ks + 8 * i + (lane >> 3)) * 2, 0, 0);
                }
            }
            __builtin_amdgcn_sched_barrier(0);
            BAR();
            GET_EPOCH();
            __builtin_amdgcn_sched_barrier(0);
#pragma unroll
            for (int i = 0; i < 4; ++i)
                *(LAS v4u*)(L + L_EX + 8192 + pp * 256 + (((4 * rg + i) ^ (pp & 15)) * 16)) = v4u{xx[4 * i], xx[4 * i + 1], xx[4 * i + 2], xx[4 * i + 3]};
            lds_arrive(CNT(2), lane);
            if (w == 4) {
                stats1((const v4u(&)[7])xr);
            } else {
                float a[8];
#pragma unroll
                for (int j = 0; j < 8; ++j) a[j] = 0.f;
#pragma unroll
                for (int i = 0; i < 14; ++i) {
                    const float cf = lo2f(cv[i]);
                    const float sc = rbf(cf * __builtin_amdgcn_rcpf(1.f + __expf(-cf)));
#pragma unroll
                    for (int j = 0; j < 4; ++j) { a[2 * j] = fmaf(sc, lo2f(xr[i][j]), a[2 * j]); a[2 * j + 1] = fmaf(sc, hi2f(xr[i][j]), a[2 * j + 1]); }
                }
                const bool b5 = lane & 32, b4 = lane & 16, b3 = lane & 8;
                float c4[4], c2[2];
#pragma unroll
                for (int j = 0; j < 4; ++j) { float keep = b5 ? a[4 + j] : a[j], send = b5 ? a[j] : a[4 + j]; c4[j] = keep + __shfl_xor(send, 32); }
#pragma unroll
                for (int j = 0; j < 2; ++j) { float keep = b4 ? c4[2 + j] : c4[j], send = b4 ? c4[j] : c4[2 + j]; c2[j] = keep + __shfl_xor(send, 16); }
                const float c1 = (b3 ? c2[1] : c2[0]) + __shfl_xor(b3 ? c2[0] : c2[1], 8);
                const int colm = slab * 64 + (lane & 7) * 8 + (lane >> 3);
                if (b < 168) __builtin_amdgcn_raw_buffer_store_b32(__float_as_uint(c1), rs, O_MODP + colm * 16 + ks * 4, 0, SC1);
            }
            lds_wait(CNT(2), 2);
            STAMP(1);
#pragma unroll
            for (int i = 0; i < 4; ++i) {
                const int p2 = 16 * (w - 4) + 4 * i + (lane >> 4), sl = lane & 15;
                v4u a = *(LAS v4u*)(L + L_EX + 8192 + p2 * 256 + ((sl ^ (p2 & 15)) * 16));
                st128(rs, O_XP + (32 * g + p2) * (S * 4) + (64 * rt + 4 * sl) * 4, a);
            }
            DRAIN();
            if (lane == 0) st64(rs, O_FX + ((w - 4) * 256 + b) * 64, basev);
            STAMP(2);
        } else {
            asm volatile("s_barrier" ::: "memory");
            const int r8 = lane >> 3, ch = lane & 7;
            const int mat = w - 6;
            const u16* wsrc = (mat ? p.wu : p.wg) + (size_t)r8 * F + nt1 * 64 + ((ch ^ (((r8 >> 1) & 1) << 2)) * 8);
            int i = (idx / 7) * 14;
#pragma unroll 2
            for (int n = 0; n < NW1A; ++n) {
                glds16(wsrc + (size_t)i * 8 * F, (unsigned)(size_t)(L + L_W1 + (i >> 1) * 4096 + mat * 2048 + (i & 1) * 1024));
                i = i == 55 ? 0 : i + 1;
            }
            STAMP(1);
            GET_EPOCH();
        }
        const int la_ = lane & 15, q_ = lane >> 4;
        lds_wait(CNT(1), 4);
        v4u KF[2][8];
        {
            const int kb = L_KL + cl * KST + ((2 + 2 * la_ + 16 * q_) & ~3) + 64 * i0b;
            const int shf = (la_ & 1) ? 0 : 16;
#pragma unroll
            for (int ii = 0; ii < 2; ++ii)
#pragma unroll
                for (int sg = 0; sg < 8; ++sg) {
                    unsigned d[5];
#pragma unroll
                    for (int j = 0; j < 5; ++j) d[j] = *(LAS unsigned*)(L + kb + ii * 64 + sg * 512 + j * 4);
#pragma unroll
                    for (int j = 0; j < 4; ++j) KF[ii][sg][j] = __builtin_amdgcn_alignbit(d[j + 1], d[j], shf);
                    asm volatile("" : "+v"(KF[ii][sg]));
                }
        }
        STAMP(3);
        if (w == 0) wait_words(rs, O_FA + (((b >> 3) & 3) * NWORK + 32 * g + (lane & 31)) * 64, base, p.ws, 1);
        BAR();
        STAMP(4);
        const int s0 = 256 * w + 16 * la_ + 4 * q_;
        int fxo;
        {
            int f;
            if (lane < 32) f = 32 * w + lane;
            else if (lane < 36) f = ((b >> 4) & 1) * 256 + 32 * g + 4 * w + (lane - 32);
            else if (lane < 52) { const int vi = (lane - 36) >> 2; f = 256 + (((vi == 3 ? 4 : vi) * 7 + g) * 4 + (lane & 3)); }
            else { const int sbx = w < 7 ? ((lane & 4) ? 21 + w : 28 + w) : 35 + nt2; f = 256 + sbx * 4 + (lane & 3); }
            fxo = O_FX + f * 64;
        }
        v4u kv4;
        if (w < 4) {
            kv4 = ld128(rs, O_KVP + b * (S * 4) + s0 * 4);
            v4u kvb = ld128(rs, O_KVP + b * (S * 4) + (s0 + 1024) * 4);
            const int ua = L_U + (2044 - s0) * 2;
            *(LAS v2u*)(L + ua) = v2u{__builtin_amdgcn_perm(kv4.z, kv4.w, 0x05040100), __builtin_amdgcn_perm(kv4.x, kv4.y, 0x05040100)};
            *(LAS v2u*)(L + ua + UST) = v2u{__builtin_amdgcn_perm(kv4.z, kv4.w, 0x07060302), __builtin_amdgcn_perm(kv4.x, kv4.y, 0x07060302)};
            *(LAS v2u*)(L + ua - 2048) = v2u{__builtin_amdgcn_perm(kvb.z, kvb.w, 0x05040100), __builtin_amdgcn_perm(kvb.x, kvb.y, 0x05040100)};
            *(LAS v2u*)(L + ua - 2048 + UST) = v2u{__builtin_amdgcn_perm(kvb.z, kvb.w, 0x07060302), __builtin_amdgcn_perm(kvb.x, kvb.y, 0x07060302)};
        }
        wait_words(rs, fxo, base, p.ws, 2);
        const v4u st1 = ld128(rs, O_ST1 + s0 * 4);
        const v4u x4 = ld128(rs, O_XP + b * (S * 4) + s0 * 4);
        if (w >= 4) kv4 = ld128(rs, O_KVP + b * (S * 4) + s0 * 4);
        const int vi_ = (lane >> 1) & 3;
        const v4u mp = ld128(rs, O_MODP + ((vi_ == 3 ? 4 : vi_) * D + 2 * b + (lane & 1)) * 16);
        const v4u cs0 = ld128(rs, O_MODP + ((w < 7 ? 21 + w : 35 + nt2) * 64 + lane) * 16);
        const v4u cs1 = ld128(rs, O_MODP + ((28 + w) * 64 + lane) * 16);
        const unsigned m_D = ((const unsigned*)p.ssmD)[b];
        __builtin_amdgcn_sched_barrier(0);
        BAR();
        STAMP(5);
        if (NW1A < 56 && w >= 6) {
            const int r8 = lane >> 3, ch = lane & 7;
            const int mat = w - 6;
            const u16* wsrc = (mat ? p.wu : p.wg) + (size_t)r8 * F + nt1 * 64 + ((ch ^ (((r8 >> 1) & 1) << 2)) * 8);
            int i = ((idx / 7) * 14 + NW1A) % 56;
#pragma unroll 2
            for (int n = NW1A; n < 56; ++n) {
                glds16(wsrc + (size_t)i * 8 * F, (unsigned)(size_t)(L + L_W1 + (i >> 1) * 4096 + mat * 2048 + (i & 1) * 1024));
                i = i == 55 ? 0 : i + 1;
            }
        }
        const int c31 = lane & 31, h_ = lane >> 5;
        unsigned WD[14][8];
        const rsrc_t rwd = __builtin_amdgcn_make_buffer_rsrc((void*)p.wd, 0, F * D * 2, 0x00020000);
        const int wdo = ((224 * w + 4 * h_) * NP + nt2 * 32 + c31) * 4;
#define LDWD(kk2, j) __builtin_amdgcn_raw_buffer_load_b32(rwd, wdo + ((j) & 3) * 896 + (16 * (kk2) + 8 * ((j) >> 2)) * 896, 0, 0)
#pragma unroll
        for (int kk2 = 0; kk2 < WD1; ++kk2)
#pragma unroll
            for (int j = 0; j < 8; ++j) WD[kk2][j] = LDWD(kk2, j);
        __builtin_amdgcn_sched_barrier(0);
        STAMP(6);
        v4f acc[8];
#pragma unroll
        for (int t = 0; t < 8; ++t) acc[t] = v4f{0.f, 0.f, 0.f, 0.f};
        const int ub = L_U + cl * UST + (2032 - 16 * la_ + 8 * q_ + 32 * i0b) * 2;
        v4u UF[2][4];
#pragma unroll
        for (int ii = 0; ii < 2; ++ii)
#pragma unroll
            for (int r = 0; r < 4; ++r) UF[ii][r] = *(LAS v4u*)(L + ub + ii * 64 - r * 512);
        __builtin_amdgcn_sched_barrier(0);
#pragma unroll
        for (int ii = 0; ii < 2; ++ii)
#pragma unroll
            for (int r = 0; r < 4; ++r)
#pragma unroll
                for (int sg = 0; sg + r < 4; ++sg) acc[sg + r] = mfma16(KF[ii][sg], UF[ii][r], acc[sg + r]);
#pragma unroll
        for (int t = 0; t < 4; ++t) *(LAS v4f*)(L + L_EX + (w * 4 + t) * 1024 + lane * 16) = acc[t];
        BAR();
        STAMP(7);
        auto finalize = [&](int tl) {
            v4f y0 = {0.f, 0.f, 0.f, 0.f}, y1 = {0.f, 0.f, 0.f, 0.f};
#pragma unroll
            for (int ww = 0; ww < 4; ++ww) {
                y0 += *(LAS v4f*)(L + L_EX + (ww * 4 + tl) * 1024 + lane * 16);
                y1 += *(LAS v4f*)(L + L_EX + ((ww + 4) * 4 + tl) * 1024 + lane * 16);
            }
            float mv = rbf((__uint_as_float(mp.x) + __uint_as_float(mp.y)) + (__uint_as_float(mp.z) + __uint_as_float(mp.w)));
            if (vi_ & 1) mv = rbf(1.f + mv);
            const float sa0 = rdl(mv, 0), sa1 = rdl(mv, 1), ca0 = rdl(mv, 2), ca1 = rdl(mv, 3), ga0 = rdl(mv, 4), ga1 = rdl(mv, 5), cb0 = rdl(mv, 6), cb1 = rdl(mv, 7);
            unsigned oa[4], ox[4];
#pragma unroll
            for (int i = 0; i < 4; ++i) {
                float a0 = y0[i], a1 = y1[i];
                const float xa = lo2f(x4[i]), xb = hi2f(x4[i]);
                const float mean = lo2f(st1[i]), rstd = __builtin_amdgcn_rcpf(hi2f(st1[i]));
                a0 = fmaf(lo2f(kv4[i]), lo2f(m_D), a0); a1 = fmaf(hi2f(kv4[i]), hi2f(m_D), a1);
                float n0 = (xa - mean) * rstd, n1 = (xb - mean) * rstd;
                n0 = fmaf(n0, ca0, sa0); n1 = fmaf(n1, ca1, sa1);
                ox[i] = pk(fmaf(n0 * a0, ga0, xa), fmaf(n1 * a1, ga1, xb));
                oa[i] = pk(lo2f(ox[i]) * cb0, hi2f(ox[i]) * cb1);
            }
            const int go = ((8 * w + (la_ >> 1)) * NP + b) * 128 + (16 * (la_ & 1) + 4 * q_) * 4;
            st128(rs, O_AP + go, v4u{oa[0], oa[1], oa[2], oa[3]});
            st128(rs, O_X1P + go, v4u{ox[0], ox[1], ox[2], ox[3]});
        };
        auto publishB = [&]() {
            DRAIN();
            if (lane < 4) add_word(p.ws, O_CB + (((w * 4 + lane) * 7 + g) * 4 + ((b >> 3) & 3)) * 64);
        };
        if (w < 4) finalize(w);
        __builtin_amdgcn_sched_barrier(0);
        STAMP(8);
#pragma unroll
        for (int ii = 0; ii < 2; ++ii)
#pragma unroll
            for (int r = 0; r < 4; ++r)
#pragma unroll
                for (int sg = 4 - r; sg + r < 8; ++sg) acc[sg + r] = mfma16(KF[ii][sg], UF[ii][r], acc[sg + r]);
        __builtin_amdgcn_sched_barrier(0);
        if (w < 4) publishB();
        __builtin_amdgcn_sched_barrier(0);
#pragma unroll
        for (int ii = 0; ii < 2; ++ii)
#pragma unroll
            for (int r = 0; r < 4; ++r) UF[ii][r] = *(LAS v4u*)(L + ub + ii * 64 - (r + 4) * 512);
        __builtin_amdgcn_sched_barrier(0);
#pragma unroll
        for (int ii = 0; ii < 2; ++ii)
#pragma unroll
            for (int r = 0; r < 4; ++r)
#pragma unroll
                for (int sg = 0; sg + r < 4; ++sg) acc[4 + sg + r] = mfma16(KF[ii][sg], UF[ii][r], acc[4 + sg + r]);
        {
            float a = rbf((__uint_as_float(cs0.x) + __uint_as_float(cs0.y)) + (__uint_as_float(cs0.z) + __uint_as_float(cs0.w)));
            float c = 1.f + rbf((__uint_as_float(cs1.x) + __uint_as_float(cs1.y)) + (__uint_as_float(cs1.z) + __uint_as_float(cs1.w)));
            const unsigned ac = pk(a, c);
            *(LAS u16*)(L + (w < 7 ? L_CS + w * 128 : L_GB) + lane * 2) = (u16)ac;
            if (w < 7) *(LAS u16*)(L + L_CS + 896 + w * 128 + lane * 2) = (u16)(ac >> 16);
        }
        BAR();
#pragma unroll
        for (int t = 0; t < 4; ++t) *(LAS v4f*)(L + L_EX + (w * 4 + t) * 1024 + lane * 16) = acc[4 + t];
        if (w >= 6) DRAIN();
        BAR();
        STAMP(9);
        const int tr_e = (lane & 15) >> 2, tr_p = lane & 3, tr_ch = (lane >> 4) & 1;
        const int trb = L_W1 + (8 * h_ + tr_e) * 128 + 32 * tr_ch + 8 * tr_p;
        const int trx = ((tr_e >> 1) & 1) * 64;
        auto wfrag = [&](int tile, int k1) -> v4u {
            const int o = trb + k1 * 4096 + (tile >> 1) * 2048 + (((tile & 1) * 64) ^ trx);
            v4s a = __builtin_amdgcn_ds_read_tr16_b64_v4i16((LAS v4s*)(L + o));
            v4s c = __builtin_amdgcn_ds_read_tr16_b64_v4i16((LAS v4s*)(L + o + 512));
            v2u a2 = __builtin_bit_cast(v2u, a), c2 = __builtin_bit_cast(v2u, c);
            return v4u{a2.x, a2.y, c2.x, c2.y};
        };
        if (w >= 4) { finalize(w - 4); lds_arrive(CNT(11), lane); publishB(); }
        else {
            v16f uacc;
#pragma unroll
            for (int i = 0; i < 16; ++i) uacc[i] = 0.f;
            const int mo = L_CS + (c31 < 16 ? 896 : 0) + 16 * h_;
#pragma unroll 1
            for (int k0 = 0; k0 < 28; k0 += 7) {
                v4u wf[7], xf[7];
#pragma unroll
                for (int i = 0; i < 7; ++i) { wf[i] = wfrag(w, k0 + i); xf[i] = *(LAS v4u*)(L + mo + (k0 + i) * 32); }
                __builtin_amdgcn_sched_barrier(0);
#pragma unroll
                for (int i = 0; i < 7; ++i) uacc = mfma32(wf[i], xf[i], uacc);
            }
            if ((lane & 15) == 0) {
#pragma unroll
                for (int g4 = 0; g4 < 4; ++g4)
                    *(LAS v4f*)(L + L_UV + ((c31 >> 4) * 128 + w * 32 + 8 * g4 + 4 * h_) * 4) = v4f{uacc[4 * g4], uacc[4 * g4 + 1], uacc[4 * g4 + 2], uacc[4 * g4 + 3]};
            }
            lds_arrive(CNT(5), lane);
        }
        STAMP(10);
        const int sb1 = cl * 32 + 4 * pn1 + (w & 3);
        if ((w & 3) == 0) {
            const int l28 = lane < 28 ? lane : 0;
            wait_words(rs, O_CB + (((cl * 4 + (pn1 >> 1)) * 4 + (x >> 1)) * 28 + l28) * 64, base + 8, p.ws, 3);
            lds_arrive(CNT(3 + cl), lane);
        } else lds_wait(CNT(3 + cl), 1);
        STAMP(11);
        v16f ga[4];
#pragma unroll
        for (int t = 0; t < 4; ++t)
#pragma unroll
            for (int i = 0; i < 16; ++i) ga[t][i] = 0.f;
        v2u st2;
        {
            constexpr int WIN = 8;
            v4u XF[WIN];
            v4u WA[2][4];
            const int xo = O_AP + sb1 * (NP * 128) + h_ * 512 + c31 * 4;
            auto ldx = [&](int k1) -> v4u {
                return v4u{ld32(rs, xo + k1 * 1024), ld32(rs, xo + k1 * 1024 + 128), ld32(rs, xo + k1 * 1024 + 256), ld32(rs, xo + k1 * 1024 + 384)};
            };
#pragma unroll
            for (int i = 0; i < WIN; ++i) XF[i] = ldx(i);
#pragma unroll
            for (int t = 0; t < 4; ++t) WA[0][t] = wfrag(t, 0);
            __builtin_amdgcn_sched_barrier(0);
#pragma unroll
            for (int k1 = 0; k1 < 28; ++k1) {
                if (k1 + 1 < 28) {
#pragma unroll
                    for (int t = 0; t < 4; ++t) WA[(k1 + 1) & 1][t] = wfrag(t, k1 + 1);
                }
                __builtin_amdgcn_sched_barrier(0);
                const v4u xf = XF[k1 % WIN];
#pragma unroll
                for (int t = 0; t < 4; ++t) ga[t] = mfma32(WA[k1 & 1][t], xf, ga[t]);
                __builtin_amdgcn_sched_barrier(0);
                if (k1 == 0) STAMP(12);
                if (k1 + WIN < 28) XF[k1 % WIN] = ldx(k1 + WIN);
                if (k1 >= WDS && k1 - WDS < 2 * (14 - WD1)) {
                    const int kk2 = WD1 + ((k1 - WDS) >> 1), jh = ((k1 - WDS) & 1) * 4;
#pragma unroll
                    for (int j = 0; j < 4; ++j) WD[kk2][jh + j] = LDWD(kk2, jh + j);
                }
                if (k1 == 24) st2 = ld64(rs, O_ST2 + (sb1 * 32 + c31) * 8);
                __builtin_amdgcn_sched_barrier(0);
            }
        }
        lds_arrive(CNT(8), lane);
        STAMP(13);
        {
#pragma unroll 1
            for (int spins = 0; !__all(st2.y == TAG32); ++spins) {
                asm volatile("" ::: "memory");
                st2 = ld64(rs, O_ST2 + (sb1 * 32 + c31) * 8);
                if (spins > SPIN_MAX) { __builtin_amdgcn_raw_buffer_store_b32(4, rs, O_TMO, 0, SC1); break; }
            }
            lds_wait(CNT(5), 4);
            STAMP(14);
            const float r2 = __builtin_amdgcn_rcpf(hi2f(st2.x)), nmr = -lo2f(st2.x) * r2;
#pragma unroll
            for (int t = 0; t < 2; ++t) {
                unsigned o[8];
#pragma unroll
                for (int g4 = 0; g4 < 4; ++g4) {
                    const int uo = L_UV + (t * 32 + 8 * g4 + 4 * h_) * 4;
                    v4f u1g = *(LAS v4f*)(L + uo), u1u = *(LAS v4f*)(L + uo + 256), u2g = *(LAS v4f*)(L + uo + 512), u2u = *(LAS v4f*)(L + uo + 768);
                    float r[4];
#pragma unroll
                    for (int i = 0; i < 4; ++i) {
                        float gg = fmaf(r2, ga[t][4 * g4 + i], fmaf(nmr, u1g[i], u2g[i]));
                        float uu = fmaf(r2, ga[2 + t][4 * g4 + i], fmaf(nmr, u1u[i], u2u[i]));
                        r[i] = silu_f(gg) * uu;
                    }
                    o[2 * g4] = pk(r[0], r[1]); o[2 * g4 + 1] = pk(r[2], r[3]);
                }
                const int go = O_G + (sb1 * 112 + 4 * nt1 + 2 * t) * 1024 + lane * 16;
                st128(rs, go, v4u{o[0], o[1], o[2], o[3]});
                st128(rs, go + 1024, v4u{o[4], o[5], o[6], o[7]});
            }
            STAMP(15);
            DRAIN();
            if (lane == 0) add_word(p.ws, O_CC + sb1 * 128);
        }
        STAMP(16);
        v4u WF[14][2];
#pragma unroll
        for (int kk2 = 0; kk2 < 14; ++kk2) {
#pragma unroll
            for (int i = 0; i < 4; ++i) {
                WF[kk2][0][i] = __builtin_amdgcn_perm(WD[kk2][2 * i + 1], WD[kk2][2 * i], 0x05040100);
                WF[kk2][1][i] = __builtin_amdgcn_perm(WD[kk2][2 * i + 1], WD[kk2][2 * i], 0x07060302);
            }
            asm volatile("" : "+v"(WF[kk2][0]), "+v"(WF[kk2][1]));
        }
#pragma unroll
        for (int st = 0; st < 2; ++st) {
            const int sb2 = st * 32 + 4 * x + rt2;
            if (w == 4 * st) { wait_words(rs, O_CC + sb2 * 128, base + 28, p.ws, 5); lds_arrive(CNT(6 + st), lane); }
            else lds_wait(CNT(6 + st), 1);
            STAMP(17 + 3 * st);
            v4u GF[14];
            const int go = O_G + (sb2 * 112 + 14 * w) * 1024 + lane * 16;
#pragma unroll
            for (int i = 0; i < 14; ++i) GF[i] = ld128(rs, go + i * 1024);
            unsigned x1v[4];
            if (st == 0) {
#pragma unroll
                for (int i = 0; i < 4; ++i) x1v[i] = ld32(rs, O_X1P + (sb2 * NP + 32 * nt2 + 4 * (tid & 7) + i) * 128 + ((tid >> 3) & 31) * 4);
            } else {
#pragma unroll
                for (int i = 0; i < 2; ++i) x1v[i] = ld32(rs, O_X1P + (sb2 * NP + 32 * nt2 + 2 * (tid & 15) + i) * 128 + (tid >> 4) * 4);
            }
            __builtin_amdgcn_sched_barrier(0);
            v16f oa[2];
#pragma unroll
            for (int i = 0; i < 16; ++i) { oa[0][i] = 0.f; oa[1][i] = 0.f; }
#pragma unroll
            for (int kk2 = 0; kk2 < 14; ++kk2) {
                oa[0] = mfma32(WF[kk2][0], GF[kk2], oa[0]);
                oa[1] = mfma32(WF[kk2][1], GF[kk2], oa[1]);
            }
            STAMP(18 + 3 * st);
            const int po = st ? 32768 + w * 8192 : (w < 4 ? L_EX + w * 8192 : (w - 4) * 8192);
            if (st == 0 && w < 4) lds_wait(CNT(11), 4); else lds_wait(CNT(8), 8);
#pragma unroll
            for (int t = 0; t < 2; ++t)
#pragma unroll
                for (int g4 = 0; g4 < 4; ++g4)
                    *(LAS v4f*)(L + po + t * 4096 + c31 * 128 + (((2 * g4 + h_) ^ (c31 & 7)) * 16)) =
                        v4f{oa[t][4 * g4], oa[t][4 * g4 + 1], oa[t][4 * g4 + 2], oa[t][4 * g4 + 3]};
            lds_arrive(CNT(9 + st), lane);
            if (st == 0) {
                if (w < 4) {
                    const int sl = tid >> 3, cg = tid & 7;
                    lds_wait(CNT(9), 8);
                    v4f r0 = {0.f, 0.f, 0.f, 0.f}, r1 = {0.f, 0.f, 0.f, 0.f};
                    const int ro = sl * 128 + ((cg ^ (sl & 7)) * 16);
#pragma unroll
                    for (int ww = 0; ww < 4; ++ww) {
                        r0 += *(LAS v4f*)(L + L_EX + ww * 8192 + ro); r1 += *(LAS v4f*)(L + L_EX + ww * 8192 + 4096 + ro);
                        r0 += *(LAS v4f*)(L + ww * 8192 + ro); r1 += *(LAS v4f*)(L + ww * 8192 + 4096 + ro);
                    }
                    const v4u gbv = *(LAS v4u*)(L + L_GB + cg * 16);
                    unsigned o[4];
#pragma unroll
                    for (int i = 0; i < 4; ++i) {
                        float a0 = r0[i], a1 = r1[i]; rb2(a0, a1);
                        a0 *= lo2f(gbv[i]); a1 *= hi2f(gbv[i]); rb2(a0, a1);
                        o[i] = pk(lo2f(x1v[i]) + a0, hi2f(x1v[i]) + a1);
                    }
                    *(v4u*)(p.out + (size_t)(sb2 * 32 + sl) * D + 64 * nt2 + 8 * cg) = v4u{o[0], o[1], o[2], o[3]};
                }
            } else {
                const int sl = tid >> 4, cg = (tid >> 1) & 7, hf = tid & 1;
                lds_wait(CNT(10), 8);
                v2f r0 = {0.f, 0.f}, r1 = {0.f, 0.f};
                const int ro = 32768 + sl * 128 + ((cg ^ (sl & 7)) * 16) + hf * 8;
#pragma unroll
                for (int ww = 0; ww < 8; ++ww) { r0 += *(LAS v2f*)(L + ww * 8192 + ro); r1 += *(LAS v2f*)(L + ww * 8192 + 4096 + ro); }
                const v2u gbv = *(LAS v2u*)(L + L_GB + (tid & 15) * 8);
                unsigned o[2];
#pragma unroll
                for (int i = 0; i < 2; ++i) {
                    float a0 = r0[i], a1 = r1[i]; rb2(a0, a1);
                    a0 *= lo2f(gbv[i]); a1 *= hi2f(gbv[i]); rb2(a0, a1);
                    o[i] = pk(lo2f(x1v[i]) + a0, hi2f(x1v[i]) + a1);
                }
                *(v2u*)(p.out + (size_t)(sb2 * 32 + sl) * D + 64 * nt2 + 4 * (tid & 15)) = v2u{o[0], o[1]};
            }
            STAMP(19 + 3 * st);
        }
    } else {
        const int hid = b - NWORK, hi = hid >> 3;
        if (w == 4) {
            v4u xr[7];
#pragma unroll
            for (int i = 0; i < 7; ++i) xr[i] = __builtin_amdgcn_raw_buffer_load_b128(rq, (8 * b + (lane >> 3)) * 896 + ((lane & 7) + 8 * i) * 16, 0, 0);
            if (lane < 4) *(LAS v4u*)(L + L_MISC + lane * 16) = v4u{0, 0, 0, 0};
            __builtin_amdgcn_sched_barrier(0);
            GET_EPOCH();
            if (lane == 0) *(LAS v2u*)(L + 65536) = v2u{(unsigned)epoch, (unsigned)(epoch >> 32)};
            stats1(xr);
            DRAIN();
            if (lane == 0) st64(rs, O_FX + b * 64, basev);
            STAMP(2);
        } else {
        }
        BAR();
        {
            const v2u e2 = *(LAS v2u*)(L + 65536);
            epoch = (u64)(unsigned)__builtin_amdgcn_readfirstlane(e2.x) | ((u64)(unsigned)__builtin_amdgcn_readfirstlane(e2.y) << 32);
            base = (epoch << 16) + 0x9E3779B97F4A0000ull;
        }
        const int st = w >> 2, t4 = tid & 255;
        const int sb = st * 32 + 4 * x + hi;
        if ((w & 3) == 0) {
            const int l28 = lane < 28 ? lane : 0;
            wait_words(rs, O_CB + (((sb >> 3) * 4 + (x >> 1)) * 28 + l28) * 64, base + 8, p.ws, 6);
            lds_arrive(CNT(st), lane);
        } else lds_wait(CNT(st), 1);
        STAMP(11);
        const int sq = t4 & 7, pg = t4 >> 3;
        const int xo = O_X1P + sb * (NP * 128) + sq * 16;
        v4u xv[7];
#pragma unroll
        for (int i = 0; i < 7; ++i) xv[i] = ld128(rs, xo + (pg + 32 * i) * 128);
        const v4u pv = ld128(rs, xo);
        float s1[4], s2[4];
#pragma unroll
        for (int j = 0; j < 4; ++j) {
            const float piv = lo2f(pv[j]);
            s1[j] = 0.f; s2[j] = 0.f;
#pragma unroll
            for (int i = 0; i < 7; ++i) {
                float a = lo2f(xv[i][j]) - piv, c = hi2f(xv[i][j]) - piv;
                s1[j] += a + c; s2[j] = fmaf(a, a, fmaf(c, c, s2[j]));
            }
        }
        const int lo_ = st * 16384;
        *(LAS v4f*)(L + lo_ + pg * 144 + sq * 16) = v4f{s1[0], s1[1], s1[2], s1[3]};
        *(LAS v4f*)(L + lo_ + 4608 + pg * 144 + sq * 16) = v4f{s2[0], s2[1], s2[2], s2[3]};
        if (t4 < 8) *(LAS v4f*)(L + lo_ + 9216 + sq * 16) = v4f{lo2f(pv[0]), lo2f(pv[1]), lo2f(pv[2]), lo2f(pv[3])};
        lds_arrive(CNT(2 + st), lane);
        lds_wait(CNT(2 + st), 4);
        {
            const int rr = (w & 3) * 8 + (lane & 7), part = (lane >> 3) & 3, arr = lane >> 5;
            float a = 0.f;
#pragma unroll
            for (int i = 0; i < 8; ++i) a += *(LAS float*)(L + lo_ + arr * 4608 + (part * 8 + i) * 144 + rr * 4);
            a += __shfl_xor(a, 8); a += __shfl_xor(a, 16);
            const float c = __shfl_xor(a, 32);
            const float piv = *(LAS float*)(L + lo_ + 9216 + rr * 4);
            const float md = a * (1.f / D);
            const float var = fmaxf(c * (1.f / D) - md * md, 0.f);
            const float sd = __builtin_amdgcn_sqrtf(rbf(rbf(var) + 1e-5f));
            if (lane < 8) st64(rs, O_ST2 + (sb * 32 + rr) * 8, v2u{pk(piv + md, sd), TAG32});
        }
        STAMP(12);
        if (hid == 7 && tid == 256) {
            const u64 e = epoch + 1;
            st64(rs, O_EPOCH, v2u{(unsigned)e, (unsigned)(e >> 32)});
        }
    }
    STAMP_OUT;
}

extern "C" void kernel_launch(void* const* d_in, const int* in_sizes, int n_in,
                              void* d_out, int out_size, void* d_ws, size_t ws_size,
                              hipStream_t stream) {
    Params p;
    p.q = (const u16*)d_in[0]; p.k = (const u16*)d_in[1]; p.v = (const u16*)d_in[2];
    p.shk = (const u16*)d_in[3]; p.ssmk = (const u16*)d_in[4]; p.ssmD = (const u16*)d_in[5];
    p.wg = (const u16*)d_in[7]; p.wu = (const u16*)d_in[8]; p.wd = (const u16*)d_in[9];
    p.c = (const u16*)d_in[10]; p.wmod = (const u16*)d_in[11];
    p.out = (u16*)d_out; p.ws = (char*)d_ws;
    (void)hipFuncSetAttribute((const void*)fused, hipFuncAttributeMaxDynamicSharedMemorySize, L_TOTAL);
    fused<<<NWORK + NHELP, 512, L_TOTAL, stream>>>(p);
}
```

```cpp
#include <hip/hip_runtime.h>
#include <hip/hip_bf16.h>
#include <stdint.h>
#define USE_NT

typedef unsigned short u16;
typedef unsigned long long u64;
typedef unsigned v4u __attribute__((ext_vector_type(4)));
typedef unsigned v2u __attribute__((ext_vector_type(2)));
typedef float v4f __attribute__((ext_vector_type(4)));
typedef float v2f __attribute__((ext_vector_type(2)));
typedef float v16f __attribute__((ext_vector_type(16)));
typedef short v8s __attribute__((ext_vector_type(8)));
typedef short v4s __attribute__((ext_vector_type(4)));
typedef __bf16 v2bf __attribute__((ext_vector_type(2)));
typedef __bf16 v8bf __attribute__((ext_vector_type(8)));
typedef v4u __attribute__((aligned(2))) v4u_a2;
#define LAS __attribute__((address_space(3)))
#define GAS __attribute__((address_space(1)))

constexpr int S = 2048, D = 448, F = 1792, NP = 224;
constexpr int NWORK = 224, NHELP = 32;

constexpr int O_EPOCH = 0;
constexpr int O_TMO = 1024;
constexpr int O_CC = 4096;
constexpr int O_ST1 = 12288;
constexpr int O_ST2 = 20480;
constexpr int O_MODP = 36864;
constexpr int O_FX = 81920;
constexpr int O_FA = 114688;
constexpr int O_CB = 172032;
constexpr int O_KVP = 229376;
constexpr int O_XP = O_KVP + NP * S * 4;
constexpr int O_AP = O_XP + NP * S * 4;
constexpr int O_X1P = O_AP + NP * S * 4;
constexpr int O_G = O_X1P + NP * S * 4;
constexpr int O_END = O_G + S * F * 2;
constexpr int O_STAMP = 32 << 20;

constexpr int L_W1 = 0;
constexpr int L_U = 114688;
constexpr int UST = 4608;
constexpr int L_EX = L_U + 2 * UST;
constexpr int L_KL = L_EX + 16384;
constexpr int KST = 4160;
constexpr int L_UV = L_EX + 32768;
constexpr int L_CS = L_UV + 1024;
constexpr int L_GB = L_CS + 1792;
constexpr int L_MISC = L_GB + 128;
constexpr int L_TOTAL = L_MISC + 64;
static_assert(L_TOTAL <= 163840, "lds");
#define CNT(i) (L + L_MISC + 4 * (i))

constexpr int SC1 = 16;
constexpr int SPIN_MAX = 1 << 18;

struct Params {
    const u16 *q, *k, *v, *shk, *ssmk, *ssmD, *wg, *wu, *wd, *c, *wmod;
    u16* out;
    char* ws;
};

__device__ __forceinline__ float lo2f(unsigned x) { return __uint_as_float(x << 16); }
__device__ __forceinline__ float hi2f(unsigned x) { return __uint_as_float(x & 0xffff0000u); }
__device__ __forceinline__ unsigned pk(float a, float b) {
    v2f x = {a, b};
    return __builtin_bit_cast(unsigned, __builtin_convertvector(x, v2bf));
}
__device__ __forceinline__ float rbf(float a) { return lo2f(pk(a, a)); }
__device__ __forceinline__ void rb2(float& a, float& b) { unsigned p = pk(a, b); a = lo2f(p); b = hi2f(p); }

#define LDIN(p) __builtin_nontemporal_load(p)
#define BAR() asm volatile("s_waitcnt lgkmcnt(0)\n\ts_barrier" ::: "memory")
#define DRAIN() asm volatile("s_waitcnt vmcnt(0)" ::: "memory")

#define STAMP(i)
#define STAMP_DECL
#define STAMP_OUT

__device__ __forceinline__ v4f mfma16(v4u a, v4u b, v4f c) {
    return __builtin_amdgcn_mfma_f32_16x16x32_bf16(__builtin_bit_cast(v8bf, a), __builtin_bit_cast(v8bf, b), c, 0, 0, 0);
}
__device__ __forceinline__ v16f mfma32(v4u a, v4u b, v16f c) {
    return __builtin_amdgcn_mfma_f32_32x32x16_bf16(__builtin_bit_cast(v8bf, a), __builtin_bit_cast(v8bf, b), c, 0, 0, 0);
}

typedef __amdgpu_buffer_rsrc_t rsrc_t;
__device__ __forceinline__ v4u ld128(rsrc_t r, int off) { return __builtin_amdgcn_raw_buffer_load_b128(r, off, 0, SC1); }
__device__ __forceinline__ v2u ld64(rsrc_t r, int off) { return __builtin_amdgcn_raw_buffer_load_b64(r, off, 0, SC1); }
__device__ __forceinline__ unsigned ld32(rsrc_t r, int off) { return __builtin_amdgcn_raw_buffer_load_b32(r, off, 0, SC1); }
__device__ __forceinline__ void st128(rsrc_t r, int off, v4u v) { __builtin_amdgcn_raw_buffer_store_b128(v, r, off, 0, SC1); }
__device__ __forceinline__ void st64(rsrc_t r, int off, v2u v) { __builtin_amdgcn_raw_buffer_store_b64(v, r, off, 0, SC1); }

__device__ __forceinline__ void wait_words(rsrc_t r, int off, u64 want, char* ws, unsigned code) {
#pragma unroll 1
    for (int spins = 0;; ++spins) {
        asm volatile("" ::: "memory");
        v2u x = ld64(r, off);
        bool ok = x.x == (unsigned)want && x.y == (unsigned)(want >> 32);
        if (__all(ok)) break;
        if (spins > SPIN_MAX) { __builtin_amdgcn_raw_buffer_store_b32(code, r, O_TMO, 0, SC1); break; }
    }
    __builtin_amdgcn_fence(__ATOMIC_ACQUIRE, "wavefront");
    asm volatile("" ::: "memory");
}
__device__ __forceinline__ void add_word(char* ws, int off) {
    __hip_atomic_fetch_add((u64*)(ws + off), 1ull, __ATOMIC_RELAXED, __HIP_MEMORY_SCOPE_AGENT);
}

__device__ __forceinline__ void glds16(const void* gsrc, unsigned lds_dst) {
    unsigned keep;
    asm volatile("s_mov_b32 %0, m0\n\ts_mov_b32 m0, %2\n\ts_nop 0\n\tglobal_load_lds_dwordx4 %1, off\n\ts_mov_b32 m0, %0"
                 : "=&s"(keep) : "v"(gsrc), "s"(lds_dst) : "memory");
}

__device__ __forceinline__ void lds_arrive(LAS char* p, int lane) {
    asm volatile("s_waitcnt lgkmcnt(0)" ::: "memory");
    if (lane == 0) __hip_atomic_fetch_add((LAS unsigned*)p, 1u, __ATOMIC_RELAXED, __HIP_MEMORY_SCOPE_WORKGROUP);
}
__device__ __forceinline__ void lds_wait(LAS char* p, unsigned n) {
#pragma unroll 1
    while (__hip_atomic_load((LAS unsigned*)p, __ATOMIC_RELAXED, __HIP_MEMORY_SCOPE_WORKGROUP) < n) __builtin_amdgcn_s_sleep(1);
    asm volatile("" ::: "memory");
}
#define NW1A 56
#define WD1 6
#define WDS 2
#define WARM_WD 0
#define AUXIN 2
__device__ __forceinline__ float rdl(float v, int l) { return __uint_as_float(__builtin_amdgcn_readlane(__float_as_uint(v), l)); }
__device__ __forceinline__ float silu_f(float g) { return g * __builtin_amdgcn_rcpf(1.f + __expf(-g)); }

__global__ __launch_bounds__(512, 2) void fused(Params p) {
    extern __shared__ __attribute__((aligned(16))) char smem[];
    LAS char* L = (LAS char*)smem;
    const int b = blockIdx.x, tid = threadIdx.x, lane = tid & 63;
    const int w = __builtin_amdgcn_readfirstlane(tid >> 6);
    const rsrc_t rs = __builtin_amdgcn_make_buffer_rsrc(p.ws, 0, O_END, 0x00020000);
    STAMP_DECL;
    STAMP(0);
    u64 epoch, base;
#define GET_EPOCH() do { asm volatile("s_load_dwordx2 %0, %1, 0x0\n\ts_waitcnt lgkmcnt(0)" : "=s"(epoch) : "s"(p.ws) : "memory"); base = (epoch << 16) + 0x9E3779B97F4A0000ull; } while (0)
#define basev (v2u{(unsigned)base, (unsigned)(base >> 32)})
    const int x = b & 7;

    const unsigned tag32 = 0;
    (void)tag32;
#define TAG32 ((unsigned)(base >> 16) ^ 0x5bd1e995u)
    auto stats1 = [&](const v4u (&xr)[7]) {
        float sum = 0.f;
#pragma unroll
        for (int i = 0; i < 7; ++i)
#pragma unroll
            for (int j = 0; j < 4; ++j) sum += lo2f(xr[i][j]) + hi2f(xr[i][j]);
        sum += __shfl_xor(sum, 1); sum += __shfl_xor(sum, 2); sum += __shfl_xor(sum, 4);
        const float m = sum * (1.f / D);
        float var = 0.f;
#pragma unroll
        for (int i = 0; i < 7; ++i)
#pragma unroll
            for (int j = 0; j < 4; ++j) { float a = lo2f(xr[i][j]) - m, c = hi2f(xr[i][j]) - m; var = fmaf(a, a, var); var = fmaf(c, c, var); }
        var += __shfl_xor(var, 1); var += __shfl_xor(var, 2); var += __shfl_xor(var, 4);
        const float sd = __builtin_amdgcn_sqrtf(rbf(rbf(var * (1.f / D)) + 1e-5f));
        if ((lane & 7) == 0) __builtin_amdgcn_raw_buffer_store_b32(pk(m, sd), rs, O_ST1 + (8 * b + (lane >> 3)) * 4, 0, SC1);
    };
    const rsrc_t rq = __builtin_amdgcn_make_buffer_rsrc((void*)p.q, 0, S * D * 2, 0x00020000);

    if (b < NWORK) {
        const int g = b >> 5, rt = b & 31, idx = b >> 3;
        const int nt1 = 7 * (x >> 1) + idx % 7, pn1 = 4 * (x & 1) + idx / 7;
        const int rt2 = idx / 7, nt2 = idx % 7;
        const int cl = w >> 2, i0b = 2 * (w & 3);
        if (w < 4) {
            unsigned kk[11], vv[8];
            const int pp = tid & 31, rg = tid >> 5;
            const int row0 = 64 * rt + 8 * rg, col = 32 * g + pp;
            const rsrc_t rk = __builtin_amdgcn_make_buffer_rsrc((void*)p.k, 0, S * D * 2, 0x00020000);
            const rsrc_t rv = __builtin_amdgcn_make_buffer_rsrc((void*)p.v, 0, S * D * 2, 0x00020000);
            const int o0 = (row0 * NP + col) * 4;
            const int oh = row0 ? o0 - 3 * 896 : 0x40000000;
#pragma unroll
            for (int i = 0; i < 3; ++i) kk[i] = __builtin_amdgcn_raw_buffer_load_b32(rk, oh + i * 896, 0, AUXIN);
#pragma unroll
            for (int i = 0; i < 8; ++i) kk[3 + i] = __builtin_amdgcn_raw_buffer_load_b32(rk, o0 + i * 896, 0, AUXIN);
#pragma unroll
            for (int i = 0; i < 8; ++i) vv[i] = __builtin_amdgcn_raw_buffer_load_b32(rv, o0 + i * 896, 0, AUXIN);
            const v4u sh = *(const v4u*)(p.shk + col * 8);
            const v4u kf0 = LDIN((const v4u*)(p.ssmk + (size_t)2 * b * S + tid * 8)), kf1 = LDIN((const v4u*)(p.ssmk + (size_t)(2 * b + 1) * S + tid * 8));
            __builtin_amdgcn_sched_barrier(0);
            asm volatile("s_barrier" ::: "memory");
            GET_EPOCH();
            __builtin_amdgcn_sched_barrier(0);
            float s0[4] = {lo2f(sh.x), hi2f(sh.x), lo2f(sh.y), hi2f(sh.y)};
            float s1[4] = {lo2f(sh.z), hi2f(sh.z), lo2f(sh.w), hi2f(sh.w)};
            unsigned o[8];
#pragma unroll
            for (int i = 0; i < 8; ++i) {
                float a0, a1;
                a0 = lo2f(kk[i]) * s0[3]; a1 = hi2f(kk[i]) * s1[3];
#pragma unroll
                for (int t = 1; t < 4; ++t) { a0 = fmaf(lo2f(kk[i + t]), s0[3 - t], a0); a1 = fmaf(hi2f(kk[i + t]), s1[3 - t], a1); }
                o[i] = pk(a0 * lo2f(vv[i]), a1 * hi2f(vv[i]));
            }
            const int la = L_EX + pp * 256;
            const int sw0 = ((2 * rg) ^ (pp & 15)) * 16;
            *(LAS v4u*)(L + la + sw0) = v4u{o[0], o[1], o[2], o[3]};
            *(LAS v4u*)(L + la + (sw0 ^ 16)) = v4u{o[4], o[5], o[6], o[7]};
            lds_arrive(CNT(0), lane);
            lds_wait(CNT(0), 4);
            STAMP(1);
#pragma unroll
            for (int i = 0; i < 2; ++i) {
                const int p2 = 8 * w + 4 * i + (lane >> 4), sl = lane & 15;
                v4u a = *(LAS v4u*)(L + L_EX + p2 * 256 + ((sl ^ (p2 & 15)) * 16));
                st128(rs, O_KVP + (32 * g + p2) * (S * 4) + (64 * rt + 4 * sl) * 4, a);
            }
            if (rt == 0 && lane < 32) st64(rs, O_CB + ((lane * 7 + g) * 4 + w) * 64, basev);
            if (b == 0 && w == 0) st64(rs, O_CC + lane * 128, basev);
            if (w == 2 && lane < 10 && b * 10 + lane < S) st64(rs, O_ST2 + (b * 10 + lane) * 8, v2u{0u, ~TAG32});
            *(LAS v4u*)(L + L_KL + 32 + tid * 16) = kf0;
            *(LAS v4u*)(L + L_KL + KST + 32 + tid * 16) = kf1;
            lds_arrive(CNT(1), lane);
            DRAIN();
            if (lane == 0) st64(rs, O_FA + (w * NWORK + b) * 64, basev);
            STAMP(2);
        } else if (w < 6) {
            unsigned xx[16];
            const int pp = tid & 31, rg = (tid >> 5) & 3;
            const int o0 = ((64 * rt + 16 * rg) * NP + 32 * g + pp) * 4;
#pragma unroll
            for (int i = 0; i < 16; ++i) xx[i] = __builtin_amdgcn_raw_buffer_load_b32(rq, o0 + i * 896, 0, 0);
            v4u xr[14]; unsigned cv[14];
            const int slab = b >> 2, ks = b & 3;
            if (w == 4) {
#pragma unroll
                for (int i = 0; i < 7; ++i) xr[i] = __builtin_amdgcn_raw_buffer_load_b128(rq, (8 * b + (lane >> 3)) * 896 + ((lane & 7) + 8 * i) * 16, 0, 0);
                *(LAS v4u*)(L + L_U + (lane >> 5) * UST + 4096 + (lane & 31) * 16) = v4u{0, 0, 0, 0};
                if (lane < 4) *(LAS v4u*)(L + L_KL + (lane >> 1) * KST + (lane & 1) * 16) = v4u{0, 0, 0, 0};
                if (lane < 4) *(LAS v4u*)(L + L_MISC + lane * 16) = v4u{0, 0, 0, 0};
            } else {
                const rsrc_t rm = __builtin_amdgcn_make_buffer_rsrc((void*)p.wmod, 0, b < 168 ? D * 6 * D * 2 : 0, 0x00020000);
                const rsrc_t rc = __builtin_amdgcn_make_buffer_rsrc((void*)p.c, 0, D * 2, 0x00020000);
#pragma unroll
                for (int i = 0; i < 14; ++i) {
                    xr[i] = __builtin_amdgcn_raw_buffer_load_b128(rm, (112 * ks + 8 * i + (lane >> 3)) * (6 * D * 2) + slab * 128 + (lane & 7) * 16, 0, AUXIN);
                    cv[i] = __builtin_amdgcn_raw_buffer_load_b16(rc, (112 * ks + 8 * i + (lane >> 3)) * 2, 0, 0);
                }
            }
            __builtin_amdgcn_sched_barrier(0);
            BAR();
            GET_EPOCH();
            __builtin_amdgcn_sched_barrier(0);
#pragma unroll
            for (int i = 0; i < 4; ++i)
                *(LAS v4u*)(L + L_EX + 8192 + pp * 256 + (((4 * rg + i) ^ (pp & 15)) * 16)) = v4u{xx[4 * i], xx[4 * i + 1], xx[4 * i + 2], xx[4 * i + 3]};
            lds_arrive(CNT(2), lane);
            if (w == 4) {
                stats1((const v4u(&)[7])xr);
            } else {
                float a[8];
#pragma unroll
                for (int j = 0; j < 8; ++j) a[j] = 0.f;
#pragma unroll
                for (int i = 0; i < 14; ++i) {
                    const float cf = lo2f(cv[i]);
                    const float sc = rbf(cf * __builtin_amdgcn_rcpf(1.f + __expf(-cf)));
#pragma unroll
                    for (int j = 0; j < 4; ++j) { a[2 * j] = fmaf(sc, lo2f(xr[i][j]), a[2 * j]); a[2 * j + 1] = fmaf(sc, hi2f(xr[i][j]), a[2 * j + 1]); }
                }
                const bool b5 = lane & 32, b4 = lane & 16, b3 = lane & 8;
                float c4[4], c2[2];
#pragma unroll
                for (int j = 0; j < 4; ++j) { float keep = b5 ? a[4 + j] : a[j], send = b5 ? a[j] : a[4 + j]; c4[j] = keep + __shfl_xor(send, 32); }
#pragma unroll
                for (int j = 0; j < 2; ++j) { float keep = b4 ? c4[2 + j] : c4[j], send = b4 ? c4[j] : c4[2 + j]; c2[j] = keep + __shfl_xor(send, 16); }
                const float c1 = (b3 ? c2[1] : c2[0]) + __shfl_xor(b3 ? c2[0] : c2[1], 8);
                const int colm = slab * 64 + (lane & 7) * 8 + (lane >> 3);
                if (b < 168) __builtin_amdgcn_raw_buffer_store_b32(__float_as_uint(c1), rs, O_MODP + colm * 16 + ks * 4, 0, SC1);
            }
            lds_wait(CNT(2), 2);
            STAMP(1);
#pragma unroll
            for (int i = 0; i < 4; ++i) {
                const int p2 = 16 * (w - 4) + 4 * i + (lane >> 4), sl = lane & 15;
                v4u a = *(LAS v4u*)(L + L_EX + 8192 + p2 * 256 + ((sl ^ (p2 & 15)) * 16));
                st128(rs, O_XP + (32 * g + p2) * (S * 4) + (64 * rt + 4 * sl) * 4, a);
            }
            DRAIN();
            if (lane == 0) st64(rs, O_FX + ((w - 4) * 256 + b) * 64, basev);
            STAMP(2);
        } else {
            asm volatile("s_barrier" ::: "memory");
            const int r8 = lane >> 3, ch = lane & 7;
            const int mat = w - 6;
            const u16* wsrc = (mat ? p.wu : p.wg) + (size_t)r8 * F + nt1 * 64 + ((ch ^ (((r8 >> 1) & 1) << 2)) * 8);
            int i = (idx / 7) * 14;
#pragma unroll 2
            for (int n = 0; n < NW1A; ++n) {
                glds16(wsrc + (size_t)i * 8 * F, (unsigned)(size_t)(L + L_W1 + (i >> 1) * 4096 + mat * 2048 + (i & 1) * 1024));
                i = i == 55 ? 0 : i + 1;
            }
            STAMP(1);
            GET_EPOCH();
        }
        const int la_ = lane & 15, q_ = lane >> 4;
        lds_wait(CNT(1), 4);
        v4u KF[2][8];
        {
            const int kb = L_KL + cl * KST + ((2 + 2 * la_ + 16 * q_) & ~3) + 64 * i0b;
            const int shf = (la_ & 1) ? 0 : 16;
#pragma unroll
            for (int ii = 0; ii < 2; ++ii)
#pragma unroll
                for (int sg = 0; sg < 8; ++sg) {
                    unsigned d[5];
#pragma unroll
                    for (int j = 0; j < 5; ++j) d[j] = *(LAS unsigned*)(L + kb + ii * 64 + sg * 512 + j * 4);
#pragma unroll
                    for (int j = 0; j < 4; ++j) KF[ii][sg][j] = __builtin_amdgcn_alignbit(d[j + 1], d[j], shf);
                    asm volatile("" : "+v"(KF[ii][sg]));
                }
        }
        STAMP(3);
        if (w == 0) wait_words(rs, O_FA + (((b >> 3) & 3) * NWORK + 32 * g + (lane & 31)) * 64, base, p.ws, 1);
        BAR();
        STAMP(4);
        const int s0 = 256 * w + 16 * la_ + 4 * q_;
        int fxo;
        {
            int f;
            if (lane < 32) f = 32 * w + lane;
            else if (lane < 36) f = ((b >> 4) & 1) * 256 + 32 * g + 4 * w + (lane - 32);
            else if (lane < 52) { const int vi = (lane - 36) >> 2; f = 256 + (((vi == 3 ? 4 : vi) * 7 + g) * 4 + (lane & 3)); }
            else { const int sbx = w < 7 ? ((lane & 4) ? 21 + w : 28 + w) : 35 + nt2; f = 256 + sbx * 4 + (lane & 3); }
            fxo = O_FX + f * 64;
        }
        v4u kv4;
        if (w < 4) {
            kv4 = ld128(rs, O_KVP + b * (S * 4) + s0 * 4);
            v4u kvb = ld128(rs, O_KVP + b * (S * 4) + (s0 + 1024) * 4);
            const int ua = L_U + (2044 - s0) * 2;
            *(LAS v2u*)(L + ua) = v2u{__builtin_amdgcn_perm(kv4.z, kv4.w, 0x05040100), __builtin_amdgcn_perm(kv4.x, kv4.y, 0x05040100)};
            *(LAS v2u*)(L + ua + UST) = v2u{__builtin_amdgcn_perm(kv4.z, kv4.w, 0x07060302), __builtin_amdgcn_perm(kv4.x, kv4.y, 0x07060302)};
            *(LAS v2u*)(L + ua - 2048) = v2u{__builtin_amdgcn_perm(kvb.z, kvb.w, 0x05040100), __builtin_amdgcn_perm(kvb.x, kvb.y, 0x05040100)};
            *(LAS v2u*)(L + ua - 2048 + UST) = v2u{__builtin_amdgcn_perm(kvb.z, kvb.w, 0x07060302), __builtin_amdgcn_perm(kvb.x, kvb.y, 0x07060302)};
        }
        wait_words(rs, fxo, base, p.ws, 2);
        const v4u st1 = ld128(rs, O_ST1 + s0 * 4);
        const v4u x4 = ld128(rs, O_XP + b * (S * 4) + s0 * 4);
        if (w >= 4) kv4 = ld128(rs, O_KVP + b * (S * 4) + s0 * 4);
        const int vi_ = (lane >> 1) & 3;
        const v4u mp = ld128(rs, O_MODP + ((vi_ == 3 ? 4 : vi_) * D + 2 * b + (lane & 1)) * 16);
        const v4u cs0 = ld128(rs, O_MODP + ((w < 7 ? 21 + w : 35 + nt2) * 64 + lane) * 16);
        const v4u cs1 = ld128(rs, O_MODP + ((28 + w) * 64 + lane) * 16);
        const unsigned m_D = ((const unsigned*)p.ssmD)[b];
        __builtin_amdgcn_sched_barrier(0);
        BAR();
        STAMP(5);
        if (NW1A < 56 && w >= 6) {
            const int r8 = lane >> 3, ch = lane & 7;
            const int mat = w - 6;
            const u16* wsrc = (mat ? p.wu : p.wg) + (size_t)r8 * F + nt1 * 64 + ((ch ^ (((r8 >> 1) & 1) << 2)) * 8);
            int i = ((idx / 7) * 14 + NW1A) % 56;
#pragma unroll 2
            for (int n = NW1A; n < 56; ++n) {
                glds16(wsrc + (size_t)i * 8 * F, (unsigned)(size_t)(L + L_W1 + (i >> 1) * 4096 + mat * 2048 + (i & 1) * 1024));
                i = i == 55 ? 0 : i + 1;
            }
        }
        const int c31 = lane & 31, h_ = lane >> 5;
        unsigned WD[14][8];
        const rsrc_t rwd = __builtin_amdgcn_make_buffer_rsrc((void*)p.wd, 0, F * D * 2, 0x00020000);
        const int wdo = ((224 * w + 4 * h_) * NP + nt2 * 32 + c31) * 4;
#define LDWD(kk2, j) __builtin_amdgcn_raw_buffer_load_b32(rwd, wdo + ((j) & 3) * 896 + (16 * (kk2) + 8 * ((j) >> 2)) * 896, 0, 0)
#pragma unroll
        for (int kk2 = 0; kk2 < WD1; ++kk2)
#pragma unroll
            for (int j = 0; j < 8; ++j) WD[kk2][j] = LDWD(kk2, j);
        __builtin_amdgcn_sched_barrier(0);
        STAMP(6);
        v4f acc[8];
#pragma unroll
        for (int t = 0; t < 8; ++t) acc[t] = v4f{0.f, 0.f, 0.f, 0.f};
        const int ub = L_U + cl * UST + (2032 - 16 * la_ + 8 * q_ + 32 * i0b) * 2;
        v4u UF[2][4];
#pragma unroll
        for (int ii = 0; ii < 2; ++ii)
#pragma unroll
            for (int r = 0; r < 4; ++r) UF[ii][r] = *(LAS v4u*)(L + ub + ii * 64 - r * 512);
        __builtin_amdgcn_sched_barrier(0);
#pragma unroll
        for (int ii = 0; ii < 2; ++ii)
#pragma unroll
            for (int r = 0; r < 4; ++r)
#pragma unroll
                for (int sg = 0; sg + r < 4; ++sg) acc[sg + r] = mfma16(KF[ii][sg], UF[ii][r], acc[sg + r]);
#pragma unroll
        for (int t = 0; t < 4; ++t) *(LAS v4f*)(L + L_EX + (w * 4 + t) * 1024 + lane * 16) = acc[t];
        BAR();
        STAMP(7);
        auto finalize = [&](int tl) {
            v4f y0 = {0.f, 0.f, 0.f, 0.f}, y1 = {0.f, 0.f, 0.f, 0.f};
#pragma unroll
            for (int ww = 0; ww < 4; ++ww) {
                y0 += *(LAS v4f*)(L + L_EX + (ww * 4 + tl) * 1024 + lane * 16);
                y1 += *(LAS v4f*)(L + L_EX + ((ww + 4) * 4 + tl) * 1024 + lane * 16);
            }
            float mv = rbf((__uint_as_float(mp.x) + __uint_as_float(mp.y)) + (__uint_as_float(mp.z) + __uint_as_float(mp.w)));
            if (vi_ & 1) mv = rbf(1.f + mv);
            const float sa0 = rdl(mv, 0), sa1 = rdl(mv, 1), ca0 = rdl(mv, 2), ca1 = rdl(mv, 3), ga0 = rdl(mv, 4), ga1 = rdl(mv, 5), cb0 = rdl(mv, 6), cb1 = rdl(mv, 7);
            unsigned oa[4], ox[4];
#pragma unroll
            for (int i = 0; i < 4; ++i) {
                float a0 = y0[i], a1 = y1[i];
                const float xa = lo2f(x4[i]), xb = hi2f(x4[i]);
                const float mean = lo2f(st1[i]), rstd = __builtin_amdgcn_rcpf(hi2f(st1[i]));
                a0 = fmaf(lo2f(kv4[i]), lo2f(m_D), a0); a1 = fmaf(hi2f(kv4[i]), hi2f(m_D), a1);
                float n0 = (xa - mean) * rstd, n1 = (xb - mean) * rstd;
                n0 = fmaf(n0, ca0, sa0); n1 = fmaf(n1, ca1, sa1);
                ox[i] = pk(fmaf(n0 * a0, ga0, xa), fmaf(n1 * a1, ga1, xb));
                oa[i] = pk(lo2f(ox[i]) * cb0, hi2f(ox[i]) * cb1);
            }
            const int go = ((8 * w + (la_ >> 1)) * NP + b) * 128 + (16 * (la_ & 1) + 4 * q_) * 4;
            st128(rs, O_AP + go, v4u{oa[0], oa[1], oa[2], oa[3]});
            st128(rs, O_X1P + go, v4u{ox[0], ox[1], ox[2], ox[3]});
        };
        auto publishB = [&]() {
            DRAIN();
            if (lane < 4) add_word(p.ws, O_CB + (((w * 4 + lane) * 7 + g) * 4 + ((b >> 3) & 3)) * 64);
        };
        if (w < 4) finalize(w);
        __builtin_amdgcn_sched_barrier(0);
        STAMP(8);
#pragma unroll
        for (int ii = 0; ii < 2; ++ii)
#pragma unroll
            for (int r = 0; r < 4; ++r)
#pragma unroll
                for (int sg = 4 - r; sg + r < 8; ++sg) acc[sg + r] = mfma16(KF[ii][sg], UF[ii][r], acc[sg + r]);
        __builtin_amdgcn_sched_barrier(0);
        if (w < 4) publishB();
        __builtin_amdgcn_sched_barrier(0);
#pragma unroll
        for (int ii = 0; ii < 2; ++ii)
#pragma unroll
            for (int r = 0; r < 4; ++r) UF[ii][r] = *(LAS v4u*)(L + ub + ii * 64 - (r + 4) * 512);
        __builtin_amdgcn_sched_barrier(0);
#pragma unroll
        for (int ii = 0; ii < 2; ++ii)
#pragma unroll
            for (int r = 0; r < 4; ++r)
#pragma unroll
                for (int sg = 0; sg + r < 4; ++sg) acc[4 + sg + r] = mfma16(KF[ii][sg], UF[ii][r], acc[4 + sg + r]);
        {
            float a = rbf((__uint_as_float(cs0.x) + __uint_as_float(cs0.y)) + (__uint_as_float(cs0.z) + __uint_as_float(cs0.w)));
            float c = 1.f + rbf((__uint_as_float(cs1.x) + __uint_as_float(cs1.y)) + (__uint_as_float(cs1.z) + __uint_as_float(cs1.w)));
            const unsigned ac = pk(a, c);
            *(LAS u16*)(L + (w < 7 ? L_CS + w * 128 : L_GB) + lane * 2) = (u16)ac;
            if (w < 7) *(LAS u16*)(L + L_CS + 896 + w * 128 + lane * 2) = (u16)(ac >> 16);
        }
        BAR();
#pragma unroll
        for (int t = 0; t < 4; ++t) *(LAS v4f*)(L + L_EX + (w * 4 + t) * 1024 + lane * 16) = acc[4 + t];
        if (w >= 6) DRAIN();
        BAR();
        STAMP(9);
        const int tr_e = (lane & 15) >> 2, tr_p = lane & 3, tr_ch = (lane >> 4) & 1;
        const int trb = L_W1 + (8 * h_ + tr_e) * 128 + 32 * tr_ch + 8 * tr_p;
        const int trx = ((tr_e >> 1) & 1) * 64;
        auto wfrag = [&](int tile, int k1) -> v4u {
            const int o = trb + k1 * 4096 + (tile >> 1) * 2048 + (((tile & 1) * 64) ^ trx);
            v4s a = __builtin_amdgcn_ds_read_tr16_b64_v4i16((LAS v4s*)(L + o));
            v4s c = __builtin_amdgcn_ds_read_tr16_b64_v4i16((LAS v4s*)(L + o + 512));
            v2u a2 = __builtin_bit_cast(v2u, a), c2 = __builtin_bit_cast(v2u, c);
            return v4u{a2.x, a2.y, c2.x, c2.y};
        };
        if (w >= 4) { finalize(w - 4); lds_arrive(CNT(11), lane); publishB(); }
        else {
            v16f uacc;
#pragma unroll
            for (int i = 0; i < 16; ++i) uacc[i] = 0.f;
            const int mo = L_CS + (c31 < 16 ? 896 : 0) + 16 * h_;
#pragma unroll 1
            for (int k0 = 0; k0 < 28; k0 += 7) {
                v4u wf[7], xf[7];
#pragma unroll
                for (int i = 0; i < 7; ++i) { wf[i] = wfrag(w, k0 + i); xf[i] = *(LAS v4u*)(L + mo + (k0 + i) * 32); }
                __builtin_amdgcn_sched_barrier(0);
#pragma unroll
                for (int i = 0; i < 7; ++i) uacc = mfma32(wf[i], xf[i], uacc);
            }
            if ((lane & 15) == 0) {
#pragma unroll
                for (int g4 = 0; g4 < 4; ++g4)
                    *(LAS v4f*)(L + L_UV + ((c31 >> 4) * 128 + w * 32 + 8 * g4 + 4 * h_) * 4) = v4f{uacc[4 * g4], uacc[4 * g4 + 1], uacc[4 * g4 + 2], uacc[4 * g4 + 3]};
            }
            lds_arrive(CNT(5), lane);
        }
        STAMP(10);
        const int sb1 = cl * 32 + 4 * pn1 + (w & 3);
        if ((w & 3) == 0) {
            const int l28 = lane < 28 ? lane : 0;
            wait_words(rs, O_CB + (((cl * 4 + (pn1 >> 1)) * 4 + (x >> 1)) * 28 + l28) * 64, base + 8, p.ws, 3);
            lds_arrive(CNT(3 + cl), lane);
        } else lds_wait(CNT(3 + cl), 1);
        STAMP(11);
        v16f ga[4];
#pragma unroll
        for (int t = 0; t < 4; ++t)
#pragma unroll
            for (int i = 0; i < 16; ++i) ga[t][i] = 0.f;
        v2u st2;
        {
            constexpr int WIN = 8;
            v4u XF[WIN];
            v4u WA[2][4];
            const int xo = O_AP + sb1 * (NP * 128) + h_ * 512 + c31 * 4;
            auto ldx = [&](int k1) -> v4u {
                return v4u{ld32(rs, xo + k1 * 1024), ld32(rs, xo + k1 * 1024 + 128), ld32(rs, xo + k1 * 1024 + 256), ld32(rs, xo + k1 * 1024 + 384)};
            };
#pragma unroll
            for (int i = 0; i < WIN; ++i) XF[i] = ldx(i);
#pragma unroll
            for (int t = 0; t < 4; ++t) WA[0][t] = wfrag(t, 0);
            __builtin_amdgcn_sched_barrier(0);
#pragma unroll
            for (int k1 = 0; k1 < 28; ++k1) {
                if (k1 + 1 < 28) {
#pragma unroll
                    for (int t = 0; t < 4; ++t) WA[(k1 + 1) & 1][t] = wfrag(t, k1 + 1);
                }
                __builtin_amdgcn_sched_barrier(0);
                const v4u xf = XF[k1 % WIN];
#pragma unroll
                for (int t = 0; t < 4; ++t) ga[t] = mfma32(WA[k1 & 1][t], xf, ga[t]);
                __builtin_amdgcn_sched_barrier(0);
                if (k1 == 0) STAMP(12);
                if (k1 + WIN < 28) XF[k1 % WIN] = ldx(k1 + WIN);
                if (k1 >= WDS && k1 - WDS < 2 * (14 - WD1)) {
                    const int kk2 = WD1 + ((k1 - WDS) >> 1), jh = ((k1 - WDS) & 1) * 4;
#pragma unroll
                    for (int j = 0; j < 4; ++j) WD[kk2][jh + j] = LDWD(kk2, jh + j);
                }
                if (k1 == 24) st2 = ld64(rs, O_ST2 + (sb1 * 32 + c31) * 8);
                __builtin_amdgcn_sched_barrier(0);
            }
        }
        lds_arrive(CNT(8), lane);
        STAMP(13);
        {
#pragma unroll 1
            for (int spins = 0; !__all(st2.y == TAG32); ++spins) {
                asm volatile("" ::: "memory");
                st2 = ld64(rs, O_ST2 + (sb1 * 32 + c31) * 8);
                if (spins > SPIN_MAX) { __builtin_amdgcn_raw_buffer_store_b32(4, rs, O_TMO, 0, SC1); break; }
            }
            lds_wait(CNT(5), 4);
            STAMP(14);
            const float r2 = __builtin_amdgcn_rcpf(hi2f(st2.x)), nmr = -lo2f(st2.x) * r2;
#pragma unroll
            for (int t = 0; t < 2; ++t) {
                unsigned o[8];
#pragma unroll
                for (int g4 = 0; g4 < 4; ++g4) {
                    const int uo = L_UV + (t * 32 + 8 * g4 + 4 * h_) * 4;
                    v4f u1g = *(LAS v4f*)(L + uo), u1u = *(LAS v4f*)(L + uo + 256), u2g = *(LAS v4f*)(L + uo + 512), u2u = *(LAS v4f*)(L + uo + 768);
                    float r[4];
#pragma unroll
                    for (int i = 0; i < 4; ++i) {
                        float gg = fmaf(r2, ga[t][4 * g4 + i], fmaf(nmr, u1g[i], u2g[i]));
                        float uu = fmaf(r2, ga[2 + t][4 * g4 + i], fmaf(nmr, u1u[i], u2u[i]));
                        r[i] = silu_f(gg) * uu;
                    }
                    o[2 * g4] = pk(r[0], r[1]); o[2 * g4 + 1] = pk(r[2], r[3]);
                }
                const int go = O_G + (sb1 * 112 + 4 * nt1 + 2 * t) * 1024 + lane * 16;
                st128(rs, go, v4u{o[0], o[1], o[2], o[3]});
                st128(rs, go + 1024, v4u{o[4], o[5], o[6], o[7]});
            }
            STAMP(15);
            DRAIN();
            if (lane == 0) add_word(p.ws, O_CC + sb1 * 128);
        }
        STAMP(16);
        v4u WF[14][2];
#pragma unroll
        for (int kk2 = 0; kk2 < 14; ++kk2) {
#pragma unroll
            for (int i = 0; i < 4; ++i) {
                WF[kk2][0][i] = __builtin_amdgcn_perm(WD[kk2][2 * i + 1], WD[kk2][2 * i], 0x05040100);
                WF[kk2][1][i] = __builtin_amdgcn_perm(WD[kk2][2 * i + 1], WD[kk2][2 * i], 0x07060302);
            }
            asm volatile("" : "+v"(WF[kk2][0]), "+v"(WF[kk2][1]));
        }
#pragma unroll
        for (int st = 0; st < 2; ++st) {
            const int sb2 = st * 32 + 4 * x + rt2;
            if (w == 4 * st) { wait_words(rs, O_CC + sb2 * 128, base + 28, p.ws, 5); lds_arrive(CNT(6 + st), lane); }
            else lds_wait(CNT(6 + st), 1);
            STAMP(17 + 3 * st);
            v4u GF[14];
            const int go = O_G + (sb2 * 112 + 14 * w) * 1024 + lane * 16;
#pragma unroll
            for (int i = 0; i < 14; ++i) GF[i] = ld128(rs, go + i * 1024);
            unsigned x1v[4];
            if (st == 0) {
#pragma unroll
                for (int i = 0; i < 4; ++i) x1v[i] = ld32(rs, O_X1P + (sb2 * NP + 32 * nt2 + 4 * (tid & 7) + i) * 128 + ((tid >> 3) & 31) * 4);
            } else {
#pragma unroll
                for (int i = 0; i < 2; ++i) x1v[i] = ld32(rs, O_X1P + (sb2 * NP + 32 * nt2 + 2 * (tid & 15) + i) * 128 + (tid >> 4) * 4);
            }
            __builtin_amdgcn_sched_barrier(0);
            v16f oa[2];
#pragma unroll
            for (int i = 0; i < 16; ++i) { oa[0][i] = 0.f; oa[1][i] = 0.f; }
#pragma unroll
            for (int kk2 = 0; kk2 < 14; ++kk2) {
                oa[0] = mfma32(WF[kk2][0], GF[kk2], oa[0]);
                oa[1] = mfma32(WF[kk2][1], GF[kk2], oa[1]);
            }
            STAMP(18 + 3 * st);
            const int po = st ? 32768 + w * 8192 : (w < 4 ? L_EX + w * 8192 : (w - 4) * 8192);
            if (st == 0 && w < 4) lds_wait(CNT(11), 4); else lds_wait(CNT(8), 8);
#pragma unroll
            for (int t = 0; t < 2; ++t)
#pragma unroll
                for (int g4 = 0; g4 < 4; ++g4)
                    *(LAS v4f*)(L + po + t * 4096 + c31 * 128 + (((2 * g4 + h_) ^ (c31 & 7)) * 16)) =
                        v4f{oa[t][4 * g4], oa[t][4 * g4 + 1], oa[t][4 * g4 + 2], oa[t][4 * g4 + 3]};
            lds_arrive(CNT(9 + st), lane);
            if (st == 0) {
                if (w < 4) {
                    const int sl = tid >> 3, cg = tid & 7;
                    lds_wait(CNT(9), 8);
                    v4f r0 = {0.f, 0.f, 0.f, 0.f}, r1 = {0.f, 0.f, 0.f, 0.f};
                    const int ro = sl * 128 + ((cg ^ (sl & 7)) * 16);
#pragma unroll
                    for (int ww = 0; ww < 4; ++ww) {
                        r0 += *(LAS v4f*)(L + L_EX + ww * 8192 + ro); r1 += *(LAS v4f*)(L + L_EX + ww * 8192 + 4096 + ro);
                        r0 += *(LAS v4f*)(L + ww * 8192 + ro); r1 += *(LAS v4f*)(L + ww * 8192 + 4096 + ro);
                    }
                    const v4u gbv = *(LAS v4u*)(L + L_GB + cg * 16);
                    unsigned o[4];
#pragma unroll
                    for (int i = 0; i < 4; ++i) {
                        float a0 = r0[i], a1 = r1[i]; rb2(a0, a1);
                        a0 *= lo2f(gbv[i]); a1 *= hi2f(gbv[i]); rb2(a0, a1);
                        o[i] = pk(lo2f(x1v[i]) + a0, hi2f(x1v[i]) + a1);
                    }
                    *(v4u*)(p.out + (size_t)(sb2 * 32 + sl) * D + 64 * nt2 + 8 * cg) = v4u{o[0], o[1], o[2], o[3]};
                }
            } else {
                const int sl = tid >> 4, cg = (tid >> 1) & 7, hf = tid & 1;
                lds_wait(CNT(10), 8);
                v2f r0 = {0.f, 0.f}, r1 = {0.f, 0.f};
                const int ro = 32768 + sl * 128 + ((cg ^ (sl & 7)) * 16) + hf * 8;
#pragma unroll
                for (int ww = 0; ww < 8; ++ww) { r0 += *(LAS v2f*)(L + ww * 8192 + ro); r1 += *(LAS v2f*)(L + ww * 8192 + 4096 + ro); }
                const v2u gbv = *(LAS v2u*)(L + L_GB + (tid & 15) * 8);
                unsigned o[2];
#pragma unroll
                for (int i = 0; i < 2; ++i) {
                    float a0 = r0[i], a1 = r1[i]; rb2(a0, a1);
                    a0 *= lo2f(gbv[i]); a1 *= hi2f(gbv[i]); rb2(a0, a1);
                    o[i] = pk(lo2f(x1v[i]) + a0, hi2f(x1v[i]) + a1);
                }
                *(v2u*)(p.out + (size_t)(sb2 * 32 + sl) * D + 64 * nt2 + 4 * (tid & 15)) = v2u{o[0], o[1]};
            }
            STAMP(19 + 3 * st);
        }
    } else {
        const int hid = b - NWORK, hi = hid >> 3;
        if (w == 4) {
            v4u xr[7];
#pragma unroll
            for (int i = 0; i < 7; ++i) xr[i] = __builtin_amdgcn_raw_buffer_load_b128(rq, (8 * b + (lane >> 3)) * 896 + ((lane & 7) + 8 * i) * 16, 0, 0);
            if (lane < 4) *(LAS v4u*)(L + L_MISC + lane * 16) = v4u{0, 0, 0, 0};
            __builtin_amdgcn_sched_barrier(0);
            GET_EPOCH();
            if (lane == 0) *(LAS v2u*)(L + 65536) = v2u{(unsigned)epoch, (unsigned)(epoch >> 32)};
            stats1(xr);
            DRAIN();
            if (lane == 0) st64(rs, O_FX + b * 64, basev);
            STAMP(2);
        } else {
        }
        BAR();
        {
            const v2u e2 = *(LAS v2u*)(L + 65536);
            epoch = (u64)(unsigned)__builtin_amdgcn_readfirstlane(e2.x) | ((u64)(unsigned)__builtin_amdgcn_readfirstlane(e2.y) << 32);
            base = (epoch << 16) + 0x9E3779B97F4A0000ull;
        }
        const int st = w >> 2, t4 = tid & 255;
        const int sb = st * 32 + 4 * x + hi;
        if ((w & 3) == 0) {
            const int l28 = lane < 28 ? lane : 0;
            wait_words(rs, O_CB + (((sb >> 3) * 4 + (x >> 1)) * 28 + l28) * 64, base + 8, p.ws, 6);
            lds_arrive(CNT(st), lane);
        } else lds_wait(CNT(st), 1);
        STAMP(11);
        const int sq = t4 & 7, pg = t4 >> 3;
        const int xo = O_X1P + sb * (NP * 128) + sq * 16;
        v4u xv[7];
#pragma unroll
        for (int i = 0; i < 7; ++i) xv[i] = ld128(rs, xo + (pg + 32 * i) * 128);
        const v4u pv = ld128(rs, xo);
        float s1[4], s2[4];
#pragma unroll
        for (int j = 0; j < 4; ++j) {
            const float piv = lo2f(pv[j]);
            s1[j] = 0.f; s2[j] = 0.f;
#pragma unroll
            for (int i = 0; i < 7; ++i) {
                float a = lo2f(xv[i][j]) - piv, c = hi2f(xv[i][j]) - piv;
                s1[j] += a + c; s2[j] = fmaf(a, a, fmaf(c, c, s2[j]));
            }
        }
        const int lo_ = st * 16384;
        *(LAS v4f*)(L + lo_ + pg * 144 + sq * 16) = v4f{s1[0], s1[1], s1[2], s1[3]};
        *(LAS v4f*)(L + lo_ + 4608 + pg * 144 + sq * 16) = v4f{s2[0], s2[1], s2[2], s2[3]};
        if (t4 < 8) *(LAS v4f*)(L + lo_ + 9216 + sq * 16) = v4f{lo2f(pv[0]), lo2f(pv[1]), lo2f(pv[2]), lo2f(pv[3])};
        lds_arrive(CNT(2 + st), lane);
        lds_wait(CNT(2 + st), 4);
        {
            const int rr = (w & 3) * 8 + (lane & 7), part = (lane >> 3) & 3, arr = lane >> 5;
            float a = 0.f;
#pragma unroll
            for (int i = 0; i < 8; ++i) a += *(LAS float*)(L + lo_ + arr * 4608 + (part * 8 + i) * 144 + rr * 4);
            a += __shfl_xor(a, 8); a += __shfl_xor(a, 16);
            const float c = __shfl_xor(a, 32);
            const float piv = *(LAS float*)(L + lo_ + 9216 + rr * 4);
            const float md = a * (1.f / D);
            const float var = fmaxf(c * (1.f / D) - md * md, 0.f);
            const float sd = __builtin_amdgcn_sqrtf(rbf(rbf(var) + 1e-5f));
            if (lane < 8) st64(rs, O_ST2 + (sb * 32 + rr) * 8, v2u{pk(piv + md, sd), TAG32});
        }
        STAMP(12);
        if (hid == 7 && tid == 256) {
            const u64 e = epoch + 1;
            st64(rs, O_EPOCH, v2u{(unsigned)e, (unsigned)(e >> 32)});
        }
    }
    STAMP_OUT;
}

__global__ __launch_bounds__(256) void k_code(float* out) {
    float a[8]; float b = 1.0001f;
    for (int i = 0; i < 8; ++i) a[i] = threadIdx.x + i;
#pragma unroll
    for (int i = 0; i < 64 / 8; ++i) {
#pragma unroll
        for (int j = 0; j < 8; ++j) asm volatile("v_fma_f32 %0, %0, %1, %1" : "+v"(a[j]) : "v"(b));
    }
    float s = 0; for (int i = 0; i < 8; ++i) s += a[i];
    out[blockIdx.x * 256 + threadIdx.x] = s;
}
extern "C" void kernel_launch(void* const* d_in, const int* in_sizes, int n_in,
                              void* d_out, int out_size, void* d_ws, size_t ws_size,
                              hipStream_t stream) {
    Params p;
    p.q = (const u16*)d_in[0]; p.k = (const u16*)d_in[1]; p.v = (const u16*)d_in[2];
    p.shk = (const u16*)d_in[3]; p.ssmk = (const u16*)d_in[4]; p.ssmD = (const u16*)d_in[5];
    p.wg = (const u16*)d_in[7]; p.wu = (const u16*)d_in[8]; p.wd = (const u16*)d_in[9];
    p.c = (const u16*)d_in[10]; p.wmod = (const u16*)d_in[11];
    p.out = (u16*)d_out; p.ws = (char*)d_ws;
    (void)hipFuncSetAttribute((const void*)fused, hipFuncAttributeMaxDynamicSharedMemorySize, L_TOTAL);
    fused<<<NWORK + NHELP, 512, L_TOTAL, stream>>>(p);
    k_code<<<256, 256, 0, stream>>>((float*)(p.ws + O_KVP));
}
```

```cpp
#include <hip/hip_runtime.h>
#include <hip/hip_bf16.h>
#include <stdint.h>
#define USE_NT
#define CODE_PF 32000
#define TLB_PF 7168

typedef unsigned short u16;
typedef unsigned long long u64;
typedef unsigned v4u __attribute__((ext_vector_type(4)));
typedef unsigned v2u __attribute__((ext_vector_type(2)));
typedef float v4f __attribute__((ext_vector_type(4)));
typedef float v2f __attribute__((ext_vector_type(2)));
typedef float v16f __attribute__((ext_vector_type(16)));
typedef short v8s __attribute__((ext_vector_type(8)));
typedef short v4s __attribute__((ext_vector_type(4)));
typedef __bf16 v2bf __attribute__((ext_vector_type(2)));
typedef __bf16 v8bf __attribute__((ext_vector_type(8)));
typedef v4u __attribute__((aligned(2))) v4u_a2;
#define LAS __attribute__((address_space(3)))
#define GAS __attribute__((address_space(1)))

constexpr int S = 2048, D = 448, F = 1792, NP = 224;
constexpr int NWORK = 224, NHELP = 32;

constexpr int O_EPOCH = 0;
constexpr int O_TMO = 1024;
constexpr int O_CC = 4096;
constexpr int O_ST1 = 12288;
constexpr int O_ST2 = 20480;
constexpr int O_MODP = 36864;
constexpr int O_FX = 81920;
constexpr int O_FA = 114688;
constexpr int O_CB = 172032;
constexpr int O_KVP = 229376;
constexpr int O_XP = O_KVP + NP * S * 4;
constexpr int O_AP = O_XP + NP * S * 4;
constexpr int O_X1P = O_AP + NP * S * 4;
constexpr int O_G = O_X1P + NP * S * 4;
constexpr int O_END = O_G + S * F * 2;
constexpr int O_STAMP = 32 << 20;

constexpr int L_W1 = 0;
constexpr int L_U = 114688;
constexpr int UST = 4608;
constexpr int L_EX = L_U + 2 * UST;
constexpr int L_KL = L_EX + 16384;
constexpr int KST = 4160;
constexpr int L_UV = L_EX + 32768;
constexpr int L_CS = L_UV + 1024;
constexpr int L_GB = L_CS + 1792;
constexpr int L_MISC = L_GB + 128;
constexpr int L_JUNK = L_MISC + 64;
constexpr int L_TOTAL = L_JUNK + 1024;
static_assert(L_TOTAL <= 163840, "lds");
#define CNT(i) (L + L_MISC + 4 * (i))

constexpr int SC1 = 16;
constexpr int SPIN_MAX = 1 << 18;

struct Params {
    const u16 *q, *k, *v, *shk, *ssmk, *ssmD, *wg, *wu, *wd, *c, *wmod;
    u16* out;
    char* ws;
};

__device__ __forceinline__ float lo2f(unsigned x) { return __uint_as_float(x << 16); }
__device__ __forceinline__ float hi2f(unsigned x) { return __uint_as_float(x & 0xffff0000u); }
__device__ __forceinline__ unsigned pk(float a, float b) {
    v2f x = {a, b};
    return __builtin_bit_cast(unsigned, __builtin_convertvector(x, v2bf));
}
__device__ __forceinline__ float rbf(float a) { return lo2f(pk(a, a)); }
__device__ __forceinline__ void rb2(float& a, float& b) { unsigned p = pk(a, b); a = lo2f(p); b = hi2f(p); }

#define LDIN(p) __builtin_nontemporal_load(p)
#define BAR() asm volatile("s_waitcnt lgkmcnt(0)\n\ts_barrier" ::: "memory")
#define DRAIN() asm volatile("s_waitcnt vmcnt(0)" ::: "memory")

#define STAMP(i)
#define STAMP_DECL
#define STAMP_OUT

__device__ __forceinline__ v4f mfma16(v4u a, v4u b, v4f c) {
    return __builtin_amdgcn_mfma_f32_16x16x32_bf16(__builtin_bit_cast(v8bf, a), __builtin_bit_cast(v8bf, b), c, 0, 0, 0);
}
__device__ __forceinline__ v16f mfma32(v4u a, v4u b, v16f c) {
    return __builtin_amdgcn_mfma_f32_32x32x16_bf16(__builtin_bit_cast(v8bf, a), __builtin_bit_cast(v8bf, b), c, 0, 0, 0);
}

typedef __amdgpu_buffer_rsrc_t rsrc_t;
__device__ __forceinline__ v4u ld128(rsrc_t r, int off) { return __builtin_amdgcn_raw_buffer_load_b128(r, off, 0, SC1); }
__device__ __forceinline__ v2u ld64(rsrc_t r, int off) { return __builtin_amdgcn_raw_buffer_load_b64(r, off, 0, SC1); }
__device__ __forceinline__ unsigned ld32(rsrc_t r, int off) { return __builtin_amdgcn_raw_buffer_load_b32(r, off, 0, SC1); }
__device__ __forceinline__ void st128(rsrc_t r, int off, v4u v) { __builtin_amdgcn_raw_buffer_store_b128(v, r, off, 0, SC1); }
__device__ __forceinline__ void st64(rsrc_t r, int off, v2u v) { __builtin_amdgcn_raw_buffer_store_b64(v, r, off, 0, SC1); }

__device__ __forceinline__ void wait_words(rsrc_t r, int off, u64 want, char* ws, unsigned code) {
#pragma unroll 1
    for (int spins = 0;; ++spins) {
        asm volatile("" ::: "memory");
        v2u x = ld64(r, off);
        bool ok = x.x == (unsigned)want && x.y == (unsigned)(want >> 32);
        if (__all(ok)) break;
        if (spins > SPIN_MAX) { __builtin_amdgcn_raw_buffer_store_b32(code, r, O_TMO, 0, SC1); break; }
    }
    __builtin_amdgcn_fence(__ATOMIC_ACQUIRE, "wavefront");
    asm volatile("" ::: "memory");
}
__device__ __forceinline__ void add_word(char* ws, int off) {
    __hip_atomic_fetch_add((u64*)(ws + off), 1ull, __ATOMIC_RELAXED, __HIP_MEMORY_SCOPE_AGENT);
}

__device__ __forceinline__ void glds16(const void* gsrc, unsigned lds_dst) {
    unsigned keep;
    asm volatile("s_mov_b32 %0, m0\n\ts_mov_b32 m0, %2\n\ts_nop 0\n\tglobal_load_lds_dwordx4 %1, off\n\ts_mov_b32 m0, %0"
                 : "=&s"(keep) : "v"(gsrc), "s"(lds_dst) : "memory");
}

__device__ __forceinline__ void lds_arrive(LAS char* p, int lane) {
    asm volatile("s_waitcnt lgkmcnt(0)" ::: "memory");
    if (lane == 0) __hip_atomic_fetch_add((LAS unsigned*)p, 1u, __ATOMIC_RELAXED, __HIP_MEMORY_SCOPE_WORKGROUP);
}
__device__ __forceinline__ void lds_wait(LAS char* p, unsigned n) {
#pragma unroll 1
    while (__hip_atomic_load((LAS unsigned*)p, __ATOMIC_RELAXED, __HIP_MEMORY_SCOPE_WORKGROUP) < n) __builtin_amdgcn_s_sleep(1);
    asm volatile("" ::: "memory");
}
#define NW1A 56
#define WD1 6
#define WDS 2
#define WARM_WD 0
#define AUXIN 2
__device__ __forceinline__ float rdl(float v, int l) { return __uint_as_float(__builtin_amdgcn_readlane(__float_as_uint(v), l)); }
__device__ __forceinline__ float silu_f(float g) { return g * __builtin_amdgcn_rcpf(1.f + __expf(-g)); }

__global__ __launch_bounds__(512, 2) void fused(Params p) {
    extern __shared__ __attribute__((aligned(16))) char smem[];
    LAS char* L = (LAS char*)smem;
    const int b = blockIdx.x, tid = threadIdx.x, lane = tid & 63;
    const int w = __builtin_amdgcn_readfirstlane(tid >> 6);
    const rsrc_t rs = __builtin_amdgcn_make_buffer_rsrc(p.ws, 0, O_END, 0x00020000);
    STAMP_DECL;
    STAMP(0);
    u64 epoch, base;
#define GET_EPOCH() do { asm volatile("s_load_dwordx2 %0, %1, 0x0\n\ts_waitcnt lgkmcnt(0)" : "=s"(epoch) : "s"(p.ws) : "memory"); base = (epoch << 16) + 0x9E3779B97F4A0000ull; } while (0)
#define basev (v2u{(unsigned)base, (unsigned)(base >> 32)})
    const int x = b & 7;
    if (w == 6) {
        glds16(p.ws + (size_t)lane * 233472 + (b >> 3) * TLB_PF, (unsigned)(size_t)(L + L_JUNK));
        const char* q = lane < 16 ? (const char*)p.out : lane < 32 ? (const char*)p.wd : lane < 48 ? (const char*)p.wg : (const char*)p.wu;
        glds16(q + (lane & 15) * 98304, (unsigned)(size_t)(L + L_JUNK));
    }
    if (w == 7) {
        u64 pc;
        asm volatile("s_getpc_b64 %0" : "=s"(pc));
        const char* cb = (const char*)(pc & ~63ull);
        const int chunk = (b >> 3);
        glds16(cb + (size_t)min(chunk * 2048 + lane * 16, CODE_PF - 16), (unsigned)(size_t)(L + L_JUNK));
        glds16(cb + (size_t)min(chunk * 2048 + 1024 + lane * 16, CODE_PF - 16), (unsigned)(size_t)(L + L_JUNK));
    }

    const unsigned tag32 = 0;
    (void)tag32;
#define TAG32 ((unsigned)(base >> 16) ^ 0x5bd1e995u)
    auto stats1 = [&](const v4u (&xr)[7]) {
        float sum = 0.f;
#pragma unroll
        for (int i = 0; i < 7; ++i)
#pragma unroll
            for (int j = 0; j < 4; ++j) sum += lo2f(xr[i][j]) + hi2f(xr[i][j]);
        sum += __shfl_xor(sum, 1); sum += __shfl_xor(sum, 2); sum += __shfl_xor(sum, 4);
        const float m = sum * (1.f / D);
        float var = 0.f;
#pragma unroll
        for (int i = 0; i < 7; ++i)
#pragma unroll
            for (int j = 0; j < 4; ++j) { float a = lo2f(xr[i][j]) - m, c = hi2f(xr[i][j]) - m; var = fmaf(a, a, var); var = fmaf(c, c, var); }
        var += __shfl_xor(var, 1); var += __shfl_xor(var, 2); var += __shfl_xor(var, 4);
        const float sd = __builtin_amdgcn_sqrtf(rbf(rbf(var * (1.f / D)) + 1e-5f));
        if ((lane & 7) == 0) __builtin_amdgcn_raw_buffer_store_b32(pk(m, sd), rs, O_ST1 + (8 * b + (lane >> 3)) * 4, 0, SC1);
    };
    const rsrc_t rq = __builtin_amdgcn_make_buffer_rsrc((void*)p.q, 0, S * D * 2, 0x00020000);

    if (b < NWORK) {
        const int g = b >> 5, rt = b & 31, idx = b >> 3;
        const int nt1 = 7 * (x >> 1) + idx % 7, pn1 = 4 * (x & 1) + idx / 7;
        const int rt2 = idx / 7, nt2 = idx % 7;
        const int cl = w >> 2, i0b = 2 * (w & 3);
        if (w < 4) {
            unsigned kk[11], vv[8];
            const int pp = tid & 31, rg = tid >> 5;
            const int row0 = 64 * rt + 8 * rg, col = 32 * g + pp;
            const rsrc_t rk = __builtin_amdgcn_make_buffer_rsrc((void*)p.k, 0, S * D * 2, 0x00020000);
            const rsrc_t rv = __builtin_amdgcn_make_buffer_rsrc((void*)p.v, 0, S * D * 2, 0x00020000);
            const int o0 = (row0 * NP + col) * 4;
            const int oh = row0 ? o0 - 3 * 896 : 0x40000000;
#pragma unroll
            for (int i = 0; i < 3; ++i) kk[i] = __builtin_amdgcn_raw_buffer_load_b32(rk, oh + i * 896, 0, AUXIN);
#pragma unroll
            for (int i = 0; i < 8; ++i) kk[3 + i] = __builtin_amdgcn_raw_buffer_load_b32(rk, o0 + i * 896, 0, AUXIN);
#pragma unroll
            for (int i = 0; i < 8; ++i) vv[i] = __builtin_amdgcn_raw_buffer_load_b32(rv, o0 + i * 896, 0, AUXIN);
            const v4u sh = *(const v4u*)(p.shk + col * 8);
            const v4u kf0 = LDIN((const v4u*)(p.ssmk + (size_t)2 * b * S + tid * 8)), kf1 = LDIN((const v4u*)(p.ssmk + (size_t)(2 * b + 1) * S + tid * 8));
            __builtin_amdgcn_sched_barrier(0);
            asm volatile("s_barrier" ::: "memory");
            GET_EPOCH();
            __builtin_amdgcn_sched_barrier(0);
            float s0[4] = {lo2f(sh.x), hi2f(sh.x), lo2f(sh.y), hi2f(sh.y)};
            float s1[4] = {lo2f(sh.z), hi2f(sh.z), lo2f(sh.w), hi2f(sh.w)};
            unsigned o[8];
#pragma unroll
            for (int i = 0; i < 8; ++i) {
                float a0, a1;
                a0 = lo2f(kk[i]) * s0[3]; a1 = hi2f(kk[i]) * s1[3];
#pragma unroll
                for (int t = 1; t < 4; ++t) { a0 = fmaf(lo2f(kk[i + t]), s0[3 - t], a0); a1 = fmaf(hi2f(kk[i + t]), s1[3 - t], a1); }
                o[i] = pk(a0 * lo2f(vv[i]), a1 * hi2f(vv[i]));
            }
            const int la = L_EX + pp * 256;
            const int sw0 = ((2 * rg) ^ (pp & 15)) * 16;
            *(LAS v4u*)(L + la + sw0) = v4u{o[0], o[1], o[2], o[3]};
            *(LAS v4u*)(L + la + (sw0 ^ 16)) = v4u{o[4], o[5], o[6], o[7]};
            lds_arrive(CNT(0), lane);
            lds_wait(CNT(0), 4);
            STAMP(1);
#pragma unroll
            for (int i = 0; i < 2; ++i) {
                const int p2 = 8 * w + 4 * i + (lane >> 4), sl = lane & 15;
                v4u a = *(LAS v4u*)(L + L_EX + p2 * 256 + ((sl ^ (p2 & 15)) * 16));
                st128(rs, O_KVP + (32 * g + p2) * (S * 4) + (64 * rt + 4 * sl) * 4, a);
            }
            if (rt == 0 && lane < 32) st64(rs, O_CB + ((lane * 7 + g) * 4 + w) * 64, basev);
            if (b == 0 && w == 0) st64(rs, O_CC + lane * 128, basev);
            if (w == 2 && lane < 10 && b * 10 + lane < S) st64(rs, O_ST2 + (b * 10 + lane) * 8, v2u{0u, ~TAG32});
            *(LAS v4u*)(L + L_KL + 32 + tid * 16) = kf0;
            *(LAS v4u*)(L + L_KL + KST + 32 + tid * 16) = kf1;
            lds_arrive(CNT(1), lane);
            DRAIN();
            if (lane == 0) st64(rs, O_FA + (w * NWORK + b) * 64, basev);
            STAMP(2);
        } else if (w < 6) {
            unsigned xx[16];
            const int pp = tid & 31, rg = (tid >> 5) & 3;
            const int o0 = ((64 * rt + 16 * rg) * NP + 32 * g + pp) * 4;
#pragma unroll
            for (int i = 0; i < 16; ++i) xx[i] = __builtin_amdgcn_raw_buffer_load_b32(rq, o0 + i * 896, 0, 0);
            v4u xr[14]; unsigned cv[14];
            const int slab = b >> 2, ks = b & 3;
            if (w == 4) {
#pragma unroll
                for (int i = 0; i < 7; ++i) xr[i] = __builtin_amdgcn_raw_buffer_load_b128(rq, (8 * b + (lane >> 3)) * 896 + ((lane & 7) + 8 * i) * 16, 0, 0);
                *(LAS v4u*)(L + L_U + (lane >> 5) * UST + 4096 + (lane & 31) * 16) = v4u{0, 0, 0, 0};
                if (lane < 4) *(LAS v4u*)(L + L_KL + (lane >> 1) * KST + (lane & 1) * 16) = v4u{0, 0, 0, 0};
                if (lane < 4) *(LAS v4u*)(L + L_MISC + lane * 16) = v4u{0, 0, 0, 0};
            } else {
                const rsrc_t rm = __builtin_amdgcn_make_buffer_rsrc((void*)p.wmod, 0, b < 168 ? D * 6 * D * 2 : 0, 0x00020000);
                const rsrc_t rc = __builtin_amdgcn_make_buffer_rsrc((void*)p.c, 0, D * 2, 0x00020000);
#pragma unroll
                for (int i = 0; i < 14; ++i) {
                    xr[i] = __builtin_amdgcn_raw_buffer_load_b128(rm, (112 * ks + 8 * i + (lane >> 3)) * (6 * D * 2) + slab * 128 + (lane & 7) * 16, 0, AUXIN);
                    cv[i] = __builtin_amdgcn_raw_buffer_load_b16(rc, (112 * ks + 8 * i + (lane >> 3)) * 2, 0, 0);
                }
            }
            __builtin_amdgcn_sched_barrier(0);
            BAR();
            GET_EPOCH();
            __builtin_amdgcn_sched_barrier(0);
#pragma unroll
            for (int i = 0; i < 4; ++i)
                *(LAS v4u*)(L + L_EX + 8192 + pp * 256 + (((4 * rg + i) ^ (pp & 15)) * 16)) = v4u{xx[4 * i], xx[4 * i + 1], xx[4 * i + 2], xx[4 * i + 3]};
            lds_arrive(CNT(2), lane);
            if (w == 4) {
                stats1((const v4u(&)[7])xr);
            } else {
                float a[8];
#pragma unroll
                for (int j = 0; j < 8; ++j) a[j] = 0.f;
#pragma unroll
                for (int i = 0; i < 14; ++i) {
                    const float cf = lo2f(cv[i]);
                    const float sc = rbf(cf * __builtin_amdgcn_rcpf(1.f + __expf(-cf)));
#pragma unroll
                    for (int j = 0; j < 4; ++j) { a[2 * j] = fmaf(sc, lo2f(xr[i][j]), a[2 * j]); a[2 * j + 1] = fmaf(sc, hi2f(xr[i][j]), a[2 * j + 1]); }
                }
                const bool b5 = lane & 32, b4 = lane & 16, b3 = lane & 8;
                float c4[4], c2[2];
#pragma unroll
                for (int j = 0; j < 4; ++j) { float keep = b5 ? a[4 + j] : a[j], send = b5 ? a[j] : a[4 + j]; c4[j] = keep + __shfl_xor(send, 32); }
#pragma unroll
                for (int j = 0; j < 2; ++j) { float keep = b4 ? c4[2 + j] : c4[j], send = b4 ? c4[j] : c4[2 + j]; c2[j] = keep + __shfl_xor(send, 16); }
                const float c1 = (b3 ? c2[1] : c2[0]) + __shfl_xor(b3 ? c2[0] : c2[1], 8);
                const int colm = slab * 64 + (lane & 7) * 8 + (lane >> 3);
                if (b < 168) __builtin_amdgcn_raw_buffer_store_b32(__float_as_uint(c1), rs, O_MODP + colm * 16 + ks * 4, 0, SC1);
            }
            lds_wait(CNT(2), 2);
            STAMP(1);
#pragma unroll
            for (int i = 0; i < 4; ++i) {
                const int p2 = 16 * (w - 4) + 4 * i + (lane >> 4), sl = lane & 15;
                v4u a = *(LAS v4u*)(L + L_EX + 8192 + p2 * 256 + ((sl ^ (p2 & 15)) * 16));
                st128(rs, O_XP + (32 * g + p2) * (S * 4) + (64 * rt + 4 * sl) * 4, a);
            }
            DRAIN();
            if (lane == 0) st64(rs, O_FX + ((w - 4) * 256 + b) * 64, basev);
            STAMP(2);
        } else {
            asm volatile("s_barrier" ::: "memory");
            const int r8 = lane >> 3, ch = lane & 7;
            const int mat = w - 6;
            const u16* wsrc = (mat ? p.wu : p.wg) + (size_t)r8 * F + nt1 * 64 + ((ch ^ (((r8 >> 1) & 1) << 2)) * 8);
            int i = (idx / 7) * 14;
#pragma unroll 2
            for (int n = 0; n < NW1A; ++n) {
                glds16(wsrc + (size_t)i * 8 * F, (unsigned)(size_t)(L + L_W1 + (i >> 1) * 4096 + mat * 2048 + (i & 1) * 1024));
                i = i == 55 ? 0 : i + 1;
            }
            STAMP(1);
            GET_EPOCH();
        }
        const int la_ = lane & 15, q_ = lane >> 4;
        lds_wait(CNT(1), 4);
        v4u KF[2][8];
        {
            const int kb = L_KL + cl * KST + ((2 + 2 * la_ + 16 * q_) & ~3) + 64 * i0b;
            const int shf = (la_ & 1) ? 0 : 16;
#pragma unroll
            for (int ii = 0; ii < 2; ++ii)
#pragma unroll
                for (int sg = 0; sg < 8; ++sg) {
                    unsigned d[5];
#pragma unroll
                    for (int j = 0; j < 5; ++j) d[j] = *(LAS unsigned*)(L + kb + ii * 64 + sg * 512 + j * 4);
#pragma unroll
                    for (int j = 0; j < 4; ++j) KF[ii][sg][j] = __builtin_amdgcn_alignbit(d[j + 1], d[j], shf);
                    asm volatile("" : "+v"(KF[ii][sg]));
                }
        }
        STAMP(3);
        if (w == 0) wait_words(rs, O_FA + (((b >> 3) & 3) * NWORK + 32 * g + (lane & 31)) * 64, base, p.ws, 1);
        BAR();
        STAMP(4);
        const int s0 = 256 * w + 16 * la_ + 4 * q_;
        int fxo;
        {
            int f;
            if (lane < 32) f = 32 * w + lane;
            else if (lane < 36) f = ((b >> 4) & 1) * 256 + 32 * g + 4 * w + (lane - 32);
            else if (lane < 52) { const int vi = (lane - 36) >> 2; f = 256 + (((vi == 3 ? 4 : vi) * 7 + g) * 4 + (lane & 3)); }
            else { const int sbx = w < 7 ? ((lane & 4) ? 21 + w : 28 + w) : 35 + nt2; f = 256 + sbx * 4 + (lane & 3); }
            fxo = O_FX + f * 64;
        }
        v4u kv4;
        if (w < 4) {
            kv4 = ld128(rs, O_KVP + b * (S * 4) + s0 * 4);
            v4u kvb = ld128(rs, O_KVP + b * (S * 4) + (s0 + 1024) * 4);
            const int ua = L_U + (2044 - s0) * 2;
            *(LAS v2u*)(L + ua) = v2u{__builtin_amdgcn_perm(kv4.z, kv4.w, 0x05040100), __builtin_amdgcn_perm(kv4.x, kv4.y, 0x05040100)};
            *(LAS v2u*)(L + ua + UST) = v2u{__builtin_amdgcn_perm(kv4.z, kv4.w, 0x07060302), __builtin_amdgcn_perm(kv4.x, kv4.y, 0x07060302)};
            *(LAS v2u*)(L + ua - 2048) = v2u{__builtin_amdgcn_perm(kvb.z, kvb.w, 0x05040100), __builtin_amdgcn_perm(kvb.x, kvb.y, 0x05040100)};
            *(LAS v2u*)(L + ua - 2048 + UST) = v2u{__builtin_amdgcn_perm(kvb.z, kvb.w, 0x07060302), __builtin_amdgcn_perm(kvb.x, kvb.y, 0x07060302)};
        }
        wait_words(rs, fxo, base, p.ws, 2);
        const v4u st1 = ld128(rs, O_ST1 + s0 * 4);
        const v4u x4 = ld128(rs, O_XP + b * (S * 4) + s0 * 4);
        if (w >= 4) kv4 = ld128(rs, O_KVP + b * (S * 4) + s0 * 4);
        const int vi_ = (lane >> 1) & 3;
        const v4u mp = ld128(rs, O_MODP + ((vi_ == 3 ? 4 : vi_) * D + 2 * b + (lane & 1)) * 16);
        const v4u cs0 = ld128(rs, O_MODP + ((w < 7 ? 21 + w : 35 + nt2) * 64 + lane) * 16);
        const v4u cs1 = ld128(rs, O_MODP + ((28 + w) * 64 + lane) * 16);
        const unsigned m_D = ((const unsigned*)p.ssmD)[b];
        __builtin_amdgcn_sched_barrier(0);
        BAR();
        STAMP(5);
        if (NW1A < 56 && w >= 6) {
            const int r8 = lane >> 3, ch = lane & 7;
            const int mat = w - 6;
            const u16* wsrc = (mat ? p.wu : p.wg) + (size_t)r8 * F + nt1 * 64 + ((ch ^ (((r8 >> 1) & 1) << 2)) * 8);
            int i = ((idx / 7) * 14 + NW1A) % 56;
#pragma unroll 2
            for (int n = NW1A; n < 56; ++n) {
                glds16(wsrc + (size_t)i * 8 * F, (unsigned)(size_t)(L + L_W1 + (i >> 1) * 4096 + mat * 2048 + (i & 1) * 1024));
                i = i == 55 ? 0 : i + 1;
            }
        }
        const int c31 = lane & 31, h_ = lane >> 5;
        unsigned WD[14][8];
        const rsrc_t rwd = __builtin_amdgcn_make_buffer_rsrc((void*)p.wd, 0, F * D * 2, 0x00020000);
        const int wdo = ((224 * w + 4 * h_) * NP + nt2 * 32 + c31) * 4;
#define LDWD(kk2, j) __builtin_amdgcn_raw_buffer_load_b32(rwd, wdo + ((j) & 3) * 896 + (16 * (kk2) + 8 * ((j) >> 2)) * 896, 0, 0)
#pragma unroll
        for (int kk2 = 0; kk2 < WD1; ++kk2)
#pragma unroll
            for (int j = 0; j < 8; ++j) WD[kk2][j] = LDWD(kk2, j);
        __builtin_amdgcn_sched_barrier(0);
        STAMP(6);
        v4f acc[8];
#pragma unroll
        for (int t = 0; t < 8; ++t) acc[t] = v4f{0.f, 0.f, 0.f, 0.f};
        const int ub = L_U + cl * UST + (2032 - 16 * la_ + 8 * q_ + 32 * i0b) * 2;
        v4u UF[2][4];
#pragma unroll
        for (int ii = 0; ii < 2; ++ii)
#pragma unroll
            for (int r = 0; r < 4; ++r) UF[ii][r] = *(LAS v4u*)(L + ub + ii * 64 - r * 512);
        __builtin_amdgcn_sched_barrier(0);
#pragma unroll
        for (int ii = 0; ii < 2; ++ii)
#pragma unroll
            for (int r = 0; r < 4; ++r)
#pragma unroll
                for (int sg = 0; sg + r < 4; ++sg) acc[sg + r] = mfma16(KF[ii][sg], UF[ii][r], acc[sg + r]);
#pragma unroll
        for (int t = 0; t < 4; ++t) *(LAS v4f*)(L + L_EX + (w * 4 + t) * 1024 + lane * 16) = acc[t];
        BAR();
        STAMP(7);
        auto finalize = [&](int tl) {
            v4f y0 = {0.f, 0.f, 0.f, 0.f}, y1 = {0.f, 0.f, 0.f, 0.f};
#pragma unroll
            for (int ww = 0; ww < 4; ++ww) {
                y0 += *(LAS v4f*)(L + L_EX + (ww * 4 + tl) * 1024 + lane * 16);
                y1 += *(LAS v4f*)(L + L_EX + ((ww + 4) * 4 + tl) * 1024 + lane * 16);
            }
            float mv = rbf((__uint_as_float(mp.x) + __uint_as_float(mp.y)) + (__uint_as_float(mp.z) + __uint_as_float(mp.w)));
            if (vi_ & 1) mv = rbf(1.f + mv);
            const float sa0 = rdl(mv, 0), sa1 = rdl(mv, 1), ca0 = rdl(mv, 2), ca1 = rdl(mv, 3), ga0 = rdl(mv, 4), ga1 = rdl(mv, 5), cb0 = rdl(mv, 6), cb1 = rdl(mv, 7);
            unsigned oa[4], ox[4];
#pragma unroll
            for (int i = 0; i < 4; ++i) {
                float a0 = y0[i], a1 = y1[i];
                const float xa = lo2f(x4[i]), xb = hi2f(x4[i]);
                const float mean = lo2f(st1[i]), rstd = __builtin_amdgcn_rcpf(hi2f(st1[i]));
                a0 = fmaf(lo2f(kv4[i]), lo2f(m_D), a0); a1 = fmaf(hi2f(kv4[i]), hi2f(m_D), a1);
                float n0 = (xa - mean) * rstd, n1 = (xb - mean) * rstd;
                n0 = fmaf(n0, ca0, sa0); n1 = fmaf(n1, ca1, sa1);
                ox[i] = pk(fmaf(n0 * a0, ga0, xa), fmaf(n1 * a1, ga1, xb));
                oa[i] = pk(lo2f(ox[i]) * cb0, hi2f(ox[i]) * cb1);
            }
            const int go = ((8 * w + (la_ >> 1)) * NP + b) * 128 + (16 * (la_ & 1) + 4 * q_) * 4;
            st128(rs, O_AP + go, v4u{oa[0], oa[1], oa[2], oa[3]});
            st128(rs, O_X1P + go, v4u{ox[0], ox[1], ox[2], ox[3]});
        };
        auto publishB = [&]() {
            DRAIN();
            if (lane < 4) add_word(p.ws, O_CB + (((w * 4 + lane) * 7 + g) * 4 + ((b >> 3) & 3)) * 64);
        };
        if (w < 4) finalize(w);
        __builtin_amdgcn_sched_barrier(0);
        STAMP(8);
#pragma unroll
        for (int ii = 0; ii < 2; ++ii)
#pragma unroll
            for (int r = 0; r < 4; ++r)
#pragma unroll
                for (int sg = 4 - r; sg + r < 8; ++sg) acc[sg + r] = mfma16(KF[ii][sg], UF[ii][r], acc[sg + r]);
        __builtin_amdgcn_sched_barrier(0);
        if (w < 4) publishB();
        __builtin_amdgcn_sched_barrier(0);
#pragma unroll
        for (int ii = 0; ii < 2; ++ii)
#pragma unroll
            for (int r = 0; r < 4; ++r) UF[ii][r] = *(LAS v4u*)(L + ub + ii * 64 - (r + 4) * 512);
        __builtin_amdgcn_sched_barrier(0);
#pragma unroll
        for (int ii = 0; ii < 2; ++ii)
#pragma unroll
            for (int r = 0; r < 4; ++r)
#pragma unroll
                for (int sg = 0; sg + r < 4; ++sg) acc[4 + sg + r] = mfma16(KF[ii][sg], UF[ii][r], acc[4 + sg + r]);
        {
            float a = rbf((__uint_as_float(cs0.x) + __uint_as_float(cs0.y)) + (__uint_as_float(cs0.z) + __uint_as_float(cs0.w)));
            float c = 1.f + rbf((__uint_as_float(cs1.x) + __uint_as_float(cs1.y)) + (__uint_as_float(cs1.z) + __uint_as_float(cs1.w)));
            const unsigned ac = pk(a, c);
            *(LAS u16*)(L + (w < 7 ? L_CS + w * 128 : L_GB) + lane * 2) = (u16)ac;
            if (w < 7) *(LAS u16*)(L + L_CS + 896 + w * 128 + lane * 2) = (u16)(ac >> 16);
        }
        BAR();
#pragma unroll
        for (int t = 0; t < 4; ++t) *(LAS v4f*)(L + L_EX + (w * 4 + t) * 1024 + lane * 16) = acc[4 + t];
        if (w >= 6) DRAIN();
        BAR();
        STAMP(9);
        const int tr_e = (lane & 15) >> 2, tr_p = lane & 3, tr_ch = (lane >> 4) & 1;
        const int trb = L_W1 + (8 * h_ + tr_e) * 128 + 32 * tr_ch + 8 * tr_p;
        const int trx = ((tr_e >> 1) & 1) * 64;
        auto wfrag = [&](int tile, int k1) -> v4u {
            const int o = trb + k1 * 4096 + (tile >> 1) * 2048 + (((tile & 1) * 64) ^ trx);
            v4s a = __builtin_amdgcn_ds_read_tr16_b64_v4i16((LAS v4s*)(L + o));
            v4s c = __builtin_amdgcn_ds_read_tr16_b64_v4i16((LAS v4s*)(L + o + 512));
            v2u a2 = __builtin_bit_cast(v2u, a), c2 = __builtin_bit_cast(v2u, c);
            return v4u{a2.x, a2.y, c2.x, c2.y};
        };
        if (w >= 4) { finalize(w - 4); lds_arrive(CNT(11), lane); publishB(); }
        else {
            v16f uacc;
#pragma unroll
            for (int i = 0; i < 16; ++i) uacc[i] = 0.f;
            const int mo = L_CS + (c31 < 16 ? 896 : 0) + 16 * h_;
#pragma unroll 1
            for (int k0 = 0; k0 < 28; k0 += 7) {
                v4u wf[7], xf[7];
#pragma unroll
                for (int i = 0; i < 7; ++i) { wf[i] = wfrag(w, k0 + i); xf[i] = *(LAS v4u*)(L + mo + (k0 + i) * 32); }
                __builtin_amdgcn_sched_barrier(0);
#pragma unroll
                for (int i = 0; i < 7; ++i) uacc = mfma32(wf[i], xf[i], uacc);
            }
            if ((lane & 15) == 0) {
#pragma unroll
                for (int g4 = 0; g4 < 4; ++g4)
                    *(LAS v4f*)(L + L_UV + ((c31 >> 4) * 128 + w * 32 + 8 * g4 + 4 * h_) * 4) = v4f{uacc[4 * g4], uacc[4 * g4 + 1], uacc[4 * g4 + 2], uacc[4 * g4 + 3]};
            }
            lds_arrive(CNT(5), lane);
        }
        STAMP(10);
        const int sb1 = cl * 32 + 4 * pn1 + (w & 3);
        if ((w & 3) == 0) {
            const int l28 = lane < 28 ? lane : 0;
            wait_words(rs, O_CB + (((cl * 4 + (pn1 >> 1)) * 4 + (x >> 1)) * 28 + l28) * 64, base + 8, p.ws, 3);
            lds_arrive(CNT(3 + cl), lane);
        } else lds_wait(CNT(3 + cl), 1);
        STAMP(11);
        v16f ga[4];
#pragma unroll
        for (int t = 0; t < 4; ++t)
#pragma unroll
            for (int i = 0; i < 16; ++i) ga[t][i] = 0.f;
        v2u st2;
        {
            constexpr int WIN = 8;
            v4u XF[WIN];
            v4u WA[2][4];
            const int xo = O_AP + sb1 * (NP * 128) + h_ * 512 + c31 * 4;
            auto ldx = [&](int k1) -> v4u {
                return v4u{ld32(rs, xo + k1 * 1024), ld32(rs, xo + k1 * 1024 + 128), ld32(rs, xo + k1 * 1024 + 256), ld32(rs, xo + k1 * 1024 + 384)};
            };
#pragma unroll
            for (int i = 0; i < WIN; ++i) XF[i] = ldx(i);
#pragma unroll
            for (int t = 0; t < 4; ++t) WA[0][t] = wfrag(t, 0);
            __builtin_amdgcn_sched_barrier(0);
#pragma unroll
            for (int k1 = 0; k1 < 28; ++k1) {
                if (k1 + 1 < 28) {
#pragma unroll
                    for (int t = 0; t < 4; ++t) WA[(k1 + 1) & 1][t] = wfrag(t, k1 + 1);
                }
                __builtin_amdgcn_sched_barrier(0);
                const v4u xf = XF[k1 % WIN];
#pragma unroll
                for (int t = 0; t < 4; ++t) ga[t] = mfma32(WA[k1 & 1][t], xf, ga[t]);
                __builtin_amdgcn_sched_barrier(0);
                if (k1 == 0) STAMP(12);
                if (k1 + WIN < 28) XF[k1 % WIN] = ldx(k1 + WIN);
                if (k1 >= WDS && k1 - WDS < 2 * (14 - WD1)) {
                    const int kk2 = WD1 + ((k1 - WDS) >> 1), jh = ((k1 - WDS) & 1) * 4;
#pragma unroll
                    for (int j = 0; j < 4; ++j) WD[kk2][jh + j] = LDWD(kk2, jh + j);
                }
                if (k1 == 24) st2 = ld64(rs, O_ST2 + (sb1 * 32 + c31) * 8);
                __builtin_amdgcn_sched_barrier(0);
            }
        }
        lds_arrive(CNT(8), lane);
        STAMP(13);
        {
#pragma unroll 1
            for (int spins = 0; !__all(st2.y == TAG32); ++spins) {
                asm volatile("" ::: "memory");
                st2 = ld64(rs, O_ST2 + (sb1 * 32 + c31) * 8);
                if (spins > SPIN_MAX) { __builtin_amdgcn_raw_buffer_store_b32(4, rs, O_TMO, 0, SC1); break; }
            }
            lds_wait(CNT(5), 4);
            STAMP(14);
            const float r2 = __builtin_amdgcn_rcpf(hi2f(st2.x)), nmr = -lo2f(st2.x) * r2;
#pragma unroll
            for (int t = 0; t < 2; ++t) {
                unsigned o[8];
#pragma unroll
                for (int g4 = 0; g4 < 4; ++g4) {
                    const int uo = L_UV + (t * 32 + 8 * g4 + 4 * h_) * 4;
                    v4f u1g = *(LAS v4f*)(L + uo), u1u = *(LAS v4f*)(L + uo + 256), u2g = *(LAS v4f*)(L + uo + 512), u2u = *(LAS v4f*)(L + uo + 768);
                    float r[4];
#pragma unroll
                    for (int i = 0; i < 4; ++i) {
                        float gg = fmaf(r2, ga[t][4 * g4 + i], fmaf(nmr, u1g[i], u2g[i]));
                        float uu = fmaf(r2, ga[2 + t][4 * g4 + i], fmaf(nmr, u1u[i], u2u[i]));
                        r[i] = silu_f(gg) * uu;
                    }
                    o[2 * g4] = pk(r[0], r[1]); o[2 * g4 + 1] = pk(r[2], r[3]);
                }
                const int go = O_G + (sb1 * 112 + 4 * nt1 + 2 * t) * 1024 + lane * 16;
                st128(rs, go, v4u{o[0], o[1], o[2], o[3]});
                st128(rs, go + 1024, v4u{o[4], o[5], o[6], o[7]});
            }
            STAMP(15);
            DRAIN();
            if (lane == 0) add_word(p.ws, O_CC + sb1 * 128);
        }
        STAMP(16);
        v4u WF[14][2];
#pragma unroll
        for (int kk2 = 0; kk2 < 14; ++kk2) {
#pragma unroll
            for (int i = 0; i < 4; ++i) {
                WF[kk2][0][i] = __builtin_amdgcn_perm(WD[kk2][2 * i + 1], WD[kk2][2 * i], 0x05040100);
                WF[kk2][1][i] = __builtin_amdgcn_perm(WD[kk2][2 * i + 1], WD[kk2][2 * i], 0x07060302);
            }
            asm volatile("" : "+v"(WF[kk2][0]), "+v"(WF[kk2][1]));
        }
#pragma unroll
        for (int st = 0; st < 2; ++st) {
            const int sb2 = st * 32 + 4 * x + rt2;
            if (w == 4 * st) { wait_words(rs, O_CC + sb2 * 128, base + 28, p.ws, 5); lds_arrive(CNT(6 + st), lane); }
            else lds_wait(CNT(6 + st), 1);
            STAMP(17 + 3 * st);
            v4u GF[14];
            const int go = O_G + (sb2 * 112 + 14 * w) * 1024 + lane * 16;
#pragma unroll
            for (int i = 0; i < 14; ++i) GF[i] = ld128(rs, go + i * 1024);
            unsigned x1v[4];
            if (st == 0) {
#pragma unroll
                for (int i = 0; i < 4; ++i) x1v[i] = ld32(rs, O_X1P + (sb2 * NP + 32 * nt2 + 4 * (tid & 7) + i) * 128 + ((tid >> 3) & 31) * 4);
            } else {
#pragma unroll
                for (int i = 0; i < 2; ++i) x1v[i] = ld32(rs, O_X1P + (sb2 * NP + 32 * nt2 + 2 * (tid & 15) + i) * 128 + (tid >> 4) * 4);
            }
            __builtin_amdgcn_sched_barrier(0);
            v16f oa[2];
#pragma unroll
            for (int i = 0; i < 16; ++i) { oa[0][i] = 0.f; oa[1][i] = 0.f; }
#pragma unroll
            for (int kk2 = 0; kk2 < 14; ++kk2) {
                oa[0] = mfma32(WF[kk2][0], GF[kk2], oa[0]);
                oa[1] = mfma32(WF[kk2][1], GF[kk2], oa[1]);
            }
            STAMP(18 + 3 * st);
            const int po = st ? 32768 + w * 8192 : (w < 4 ? L_EX + w * 8192 : (w - 4) * 8192);
            if (st == 0 && w < 4) lds_wait(CNT(11), 4); else lds_wait(CNT(8), 8);
#pragma unroll
            for (int t = 0; t < 2; ++t)
#pragma unroll
                for (int g4 = 0; g4 < 4; ++g4)
                    *(LAS v4f*)(L + po + t * 4096 + c31 * 128 + (((2 * g4 + h_) ^ (c31 & 7)) * 16)) =
                        v4f{oa[t][4 * g4], oa[t][4 * g4 + 1], oa[t][4 * g4 + 2], oa[t][4 * g4 + 3]};
            lds_arrive(CNT(9 + st), lane);
            if (st == 0) {
                if (w < 4) {
                    const int sl = tid >> 3, cg = tid & 7;
                    lds_wait(CNT(9), 8);
                    v4f r0 = {0.f, 0.f, 0.f, 0.f}, r1 = {0.f, 0.f, 0.f, 0.f};
                    const int ro = sl * 128 + ((cg ^ (sl & 7)) * 16);
#pragma unroll
                    for (int ww = 0; ww < 4; ++ww) {
                        r0 += *(LAS v4f*)(L + L_EX + ww * 8192 + ro); r1 += *(LAS v4f*)(L + L_EX + ww * 8192 + 4096 + ro);
                        r0 += *(LAS v4f*)(L + ww * 8192 + ro); r1 += *(LAS v4f*)(L + ww * 8192 + 4096 + ro);
                    }
                    const v4u gbv = *(LAS v4u*)(L + L_GB + cg * 16);
                    unsigned o[4];
#pragma unroll
                    for (int i = 0; i < 4; ++i) {
                        float a0 = r0[i], a1 = r1[i]; rb2(a0, a1);
                        a0 *= lo2f(gbv[i]); a1 *= hi2f(gbv[i]); rb2(a0, a1);
                        o[i] = pk(lo2f(x1v[i]) + a0, hi2f(x1v[i]) + a1);
                    }
                    *(v4u*)(p.out + (size_t)(sb2 * 32 + sl) * D + 64 * nt2 + 8 * cg) = v4u{o[0], o[1], o[2], o[3]};
                }
            } else {
                const int sl = tid >> 4, cg = (tid >> 1) & 7, hf = tid & 1;
                lds_wait(CNT(10), 8);
                v2f r0 = {0.f, 0.f}, r1 = {0.f, 0.f};
                const int ro = 32768 + sl * 128 + ((cg ^ (sl & 7)) * 16) + hf * 8;
#pragma unroll
                for (int ww = 0; ww < 8; ++ww) { r0 += *(LAS v2f*)(L + ww * 8192 + ro); r1 += *(LAS v2f*)(L + ww * 8192 + 4096 + ro); }
                const v2u gbv = *(LAS v2u*)(L + L_GB + (tid & 15) * 8);
                unsigned o[2];
#pragma unroll
                for (int i = 0; i < 2; ++i) {
                    float a0 = r0[i], a1 = r1[i]; rb2(a0, a1);
                    a0 *= lo2f(gbv[i]); a1 *= hi2f(gbv[i]); rb2(a0, a1);
                    o[i] = pk(lo2f(x1v[i]) + a0, hi2f(x1v[i]) + a1);
                }
                *(v2u*)(p.out + (size_t)(sb2 * 32 + sl) * D + 64 * nt2 + 4 * (tid & 15)) = v2u{o[0], o[1]};
            }
            STAMP(19 + 3 * st);
        }
    } else {
        const int hid = b - NWORK, hi = hid >> 3;
        if (w == 4) {
            v4u xr[7];
#pragma unroll
            for (int i = 0; i < 7; ++i) xr[i] = __builtin_amdgcn_raw_buffer_load_b128(rq, (8 * b + (lane >> 3)) * 896 + ((lane & 7) + 8 * i) * 16, 0, 0);
            if (lane < 4) *(LAS v4u*)(L + L_MISC + lane * 16) = v4u{0, 0, 0, 0};
            __builtin_amdgcn_sched_barrier(0);
            GET_EPOCH();
            if (lane == 0) *(LAS v2u*)(L + 65536) = v2u{(unsigned)epoch, (unsigned)(epoch >> 32)};
            stats1(xr);
            DRAIN();
            if (lane == 0) st64(rs, O_FX + b * 64, basev);
            STAMP(2);
        } else {
        }
        BAR();
        {
            const v2u e2 = *(LAS v2u*)(L + 65536);
            epoch = (u64)(unsigned)__builtin_amdgcn_readfirstlane(e2.x) | ((u64)(unsigned)__builtin_amdgcn_readfirstlane(e2.y) << 32);
            base = (epoch << 16) + 0x9E3779B97F4A0000ull;
        }
        const int st = w >> 2, t4 = tid & 255;
        const int sb = st * 32 + 4 * x + hi;
        if ((w & 3) == 0) {
            const int l28 = lane < 28 ? lane : 0;
            wait_words(rs, O_CB + (((sb >> 3) * 4 + (x >> 1)) * 28 + l28) * 64, base + 8, p.ws, 6);
            lds_arrive(CNT(st), lane);
        } else lds_wait(CNT(st), 1);
        STAMP(11);
        const int sq = t4 & 7, pg = t4 >> 3;
        const int xo = O_X1P + sb * (NP * 128) + sq * 16;
        v4u xv[7];
#pragma unroll
        for (int i = 0; i < 7; ++i) xv[i] = ld128(rs, xo + (pg + 32 * i) * 128);
        const v4u pv = ld128(rs, xo);
        float s1[4], s2[4];
#pragma unroll
        for (int j = 0; j < 4; ++j) {
            const float piv = lo2f(pv[j]);
            s1[j] = 0.f; s2[j] = 0.f;
#pragma unroll
            for (int i = 0; i < 7; ++i) {
                float a = lo2f(xv[i][j]) - piv, c = hi2f(xv[i][j]) - piv;
                s1[j] += a + c; s2[j] = fmaf(a, a, fmaf(c, c, s2[j]));
            }
        }
        const int lo_ = st * 16384;
        *(LAS v4f*)(L + lo_ + pg * 144 + sq * 16) = v4f{s1[0], s1[1], s1[2], s1[3]};
        *(LAS v4f*)(L + lo_ + 4608 + pg * 144 + sq * 16) = v4f{s2[0], s2[1], s2[2], s2[3]};
        if (t4 < 8) *(LAS v4f*)(L + lo_ + 9216 + sq * 16) = v4f{lo2f(pv[0]), lo2f(pv[1]), lo2f(pv[2]), lo2f(pv[3])};
        lds_arrive(CNT(2 + st), lane);
        lds_wait(CNT(2 + st), 4);
        {
            const int rr = (w & 3) * 8 + (lane & 7), part = (lane >> 3) & 3, arr = lane >> 5;
            float a = 0.f;
#pragma unroll
            for (int i = 0; i < 8; ++i) a += *(LAS float*)(L + lo_ + arr * 4608 + (part * 8 + i) * 144 + rr * 4);
            a += __shfl_xor(a, 8); a += __shfl_xor(a, 16);
            const float c = __shfl_xor(a, 32);
            const float piv = *(LAS float*)(L + lo_ + 9216 + rr * 4);
            const float md = a * (1.f / D);
            const float var = fmaxf(c * (1.f / D) - md * md, 0.f);
            const float sd = __builtin_amdgcn_sqrtf(rbf(rbf(var) + 1e-5f));
            if (lane < 8) st64(rs, O_ST2 + (sb * 32 + rr) * 8, v2u{pk(piv + md, sd), TAG32});
        }
        STAMP(12);
        if (hid == 7 && tid == 256) {
            const u64 e = epoch + 1;
            st64(rs, O_EPOCH, v2u{(unsigned)e, (unsigned)(e >> 32)});
        }
    }
    STAMP_OUT;
}

extern "C" void kernel_launch(void* const* d_in, const int* in_sizes, int n_in,
                              void* d_out, int out_size, void* d_ws, size_t ws_size,
                              hipStream_t stream) {
    Params p;
    p.q = (const u16*)d_in[0]; p.k = (const u16*)d_in[1]; p.v = (const u16*)d_in[2];
    p.shk = (const u16*)d_in[3]; p.ssmk = (const u16*)d_in[4]; p.ssmD = (const u16*)d_in[5];
    p.wg = (const u16*)d_in[7]; p.wu = (const u16*)d_in[8]; p.wd = (const u16*)d_in[9];
    p.c = (const u16*)d_in[10]; p.wmod = (const u16*)d_in[11];
    p.out = (u16*)d_out; p.ws = (char*)d_ws;
    (void)hipFuncSetAttribute((const void*)fused, hipFuncAttributeMaxDynamicSharedMemorySize, L_TOTAL);
    fused<<<NWORK + NHELP, 512, L_TOTAL, stream>>>(p);
}
```

```cpp
#include <hip/hip_runtime.h>
#include <hip/hip_bf16.h>
#include <stdint.h>
#define USE_NT
#define CODE_PF 32000

typedef unsigned short u16;
typedef unsigned long long u64;
typedef unsigned v4u __attribute__((ext_vector_type(4)));
typedef unsigned v2u __attribute__((ext_vector_type(2)));
typedef float v4f __attribute__((ext_vector_type(4)));
typedef float v2f __attribute__((ext_vector_type(2)));
typedef float v16f __attribute__((ext_vector_type(16)));
typedef short v8s __attribute__((ext_vector_type(8)));
typedef short v4s __attribute__((ext_vector_type(4)));
typedef __bf16 v2bf __attribute__((ext_vector_type(2)));
typedef __bf16 v8bf __attribute__((ext_vector_type(8)));
typedef v4u __attribute__((aligned(2))) v4u_a2;
#define LAS __attribute__((address_space(3)))
#define GAS __attribute__((address_space(1)))

constexpr int S = 2048, D = 448, F = 1792, NP = 224;
constexpr int NWORK = 224, NHELP = 32;

constexpr int O_EPOCH = 0;
constexpr int O_TMO = 1024;
constexpr int O_CC = 4096;
constexpr int O_ST1 = 12288;
constexpr int O_ST2 = 20480;
constexpr int O_MODP = 36864;
constexpr int O_FX = 81920;
constexpr int O_FA = 114688;
constexpr int O_CB = 172032;
constexpr int O_KVP = 229376;
constexpr int O_XP = O_KVP + NP * S * 4;
constexpr int O_AP = O_XP + NP * S * 4;
constexpr int O_X1P = O_AP + NP * S * 4;
constexpr int O_G = O_X1P + NP * S * 4;
constexpr int O_END = O_G + S * F * 2;
constexpr int O_STAMP = 32 << 20;

constexpr int L_W1 = 0;
constexpr int L_U = 114688;
constexpr int UST = 4608;
constexpr int L_EX = L_U + 2 * UST;
constexpr int L_KL = L_EX + 16384;
constexpr int KST = 4160;
constexpr int L_UV = L_EX + 32768;
constexpr int L_CS = L_UV + 1024;
constexpr int L_GB = L_CS + 1792;
constexpr int L_MISC = L_GB + 128;
constexpr int L_JUNK = L_MISC + 64;
constexpr int L_TOTAL = L_JUNK + 1024;
static_assert(L_TOTAL <= 163840, "lds");
#define CNT(i) (L + L_MISC + 4 * (i))

constexpr int SC1 = 16;
constexpr int SPIN_MAX = 1 << 18;

struct Params {
    const u16 *q, *k, *v, *shk, *ssmk, *ssmD, *wg, *wu, *wd, *c, *wmod;
    u16* out;
    char* ws;
};

__device__ __forceinline__ float lo2f(unsigned x) { return __uint_as_float(x << 16); }
__device__ __forceinline__ float hi2f(unsigned x) { return __uint_as_float(x & 0xffff0000u); }
__device__ __forceinline__ unsigned pk(float a, float b) {
    v2f x = {a, b};
    return __builtin_bit_cast(unsigned, __builtin_convertvector(x, v2bf));
}
__device__ __forceinline__ float rbf(float a) { return lo2f(pk(a, a)); }
__device__ __forceinline__ void rb2(float& a, float& b) { unsigned p = pk(a, b); a = lo2f(p); b = hi2f(p); }

#define LDIN(p) __builtin_nontemporal_load(p)
#define BAR() asm volatile("s_waitcnt lgkmcnt(0)\n\ts_barrier" ::: "memory")
#define DRAIN() asm volatile("s_waitcnt vmcnt(0)" ::: "memory")

#define STAMP(i)
#define STAMP_DECL
#define STAMP_OUT

__device__ __forceinline__ v4f mfma16(v4u a, v4u b, v4f c) {
    return __builtin_amdgcn_mfma_f32_16x16x32_bf16(__builtin_bit_cast(v8bf, a), __builtin_bit_cast(v8bf, b), c, 0, 0, 0);
}
__device__ __forceinline__ v16f mfma32(v4u a, v4u b, v16f c) {
    return __builtin_amdgcn_mfma_f32_32x32x16_bf16(__builtin_bit_cast(v8bf, a), __builtin_bit_cast(v8bf, b), c, 0, 0, 0);
}

typedef __amdgpu_buffer_rsrc_t rsrc_t;
__device__ __forceinline__ v4u ld128(rsrc_t r, int off) { return __builtin_amdgcn_raw_buffer_load_b128(r, off, 0, SC1); }
__device__ __forceinline__ v2u ld64(rsrc_t r, int off) { return __builtin_amdgcn_raw_buffer_load_b64(r, off, 0, SC1); }
__device__ __forceinline__ unsigned ld32(rsrc_t r, int off) { return __builtin_amdgcn_raw_buffer_load_b32(r, off, 0, SC1); }
__device__ __forceinline__ void st128(rsrc_t r, int off, v4u v) { __builtin_amdgcn_raw_buffer_store_b128(v, r, off, 0, SC1); }
__device__ __forceinline__ void st64(rsrc_t r, int off, v2u v) { __builtin_amdgcn_raw_buffer_store_b64(v, r, off, 0, SC1); }

__device__ __forceinline__ void wait_words(rsrc_t r, int off, u64 want, char* ws, unsigned code) {
#pragma unroll 1
    for (int spins = 0;; ++spins) {
        asm volatile("" ::: "memory");
        v2u x = ld64(r, off);
        bool ok = x.x == (unsigned)want && x.y == (unsigned)(want >> 32);
        if (__all(ok)) break;
        if (spins > SPIN_MAX) { __builtin_amdgcn_raw_buffer_store_b32(code, r, O_TMO, 0, SC1); break; }
    }
    __builtin_amdgcn_fence(__ATOMIC_ACQUIRE, "wavefront");
    asm volatile("" ::: "memory");
}
__device__ __forceinline__ void add_word(char* ws, int off) {
    __hip_atomic_fetch_add((u64*)(ws + off), 1ull, __ATOMIC_RELAXED, __HIP_MEMORY_SCOPE_AGENT);
}

__device__ __forceinline__ void glds16(const void* gsrc, unsigned lds_dst) {
    unsigned keep;
    asm volatile("s_mov_b32 %0, m0\n\ts_mov_b32 m0, %2\n\ts_nop 0\n\tglobal_load_lds_dwordx4 %1, off\n\ts_mov_b32 m0, %0"
                 : "=&s"(keep) : "v"(gsrc), "s"(lds_dst) : "memory");
}

__device__ __forceinline__ void lds_arrive(LAS char* p, int lane) {
    asm volatile("s_waitcnt lgkmcnt(0)" ::: "memory");
    if (lane == 0) __hip_atomic_fetch_add((LAS unsigned*)p, 1u, __ATOMIC_RELAXED, __HIP_MEMORY_SCOPE_WORKGROUP);
}
__device__ __forceinline__ void lds_wait(LAS char* p, unsigned n) {
#pragma unroll 1
    while (__hip_atomic_load((LAS unsigned*)p, __ATOMIC_RELAXED, __HIP_MEMORY_SCOPE_WORKGROUP) < n) __builtin_amdgcn_s_sleep(1);
    asm volatile("" ::: "memory");
}
#define TLB_PF 0
#define NW1A 56
#define WD1 6
#define WDS 2
#define WARM_WD 0
#define AUXIN 2
__device__ __forceinline__ float rdl(float v, int l) { return __uint_as_float(__builtin_amdgcn_readlane(__float_as_uint(v), l)); }
__device__ __forceinline__ float silu_f(float g) { return g * __builtin_amdgcn_rcpf(1.f + __expf(-g)); }

__global__ __launch_bounds__(512, 2) void fused(Params p) {
    extern __shared__ __attribute__((aligned(16))) char smem[];
    LAS char* L = (LAS char*)smem;
    const int b = blockIdx.x, tid = threadIdx.x, lane = tid & 63;
    const int w = __builtin_amdgcn_readfirstlane(tid >> 6);
    const rsrc_t rs = __builtin_amdgcn_make_buffer_rsrc(p.ws, 0, O_END, 0x00020000);
    STAMP_DECL;
    STAMP(0);
    u64 epoch, base;
#define GET_EPOCH() do { asm volatile("s_load_dwordx2 %0, %1, 0x0\n\ts_waitcnt lgkmcnt(0)" : "=s"(epoch) : "s"(p.ws) : "memory"); base = (epoch << 16) + 0x9E3779B97F4A0000ull; } while (0)
#define basev (v2u{(unsigned)base, (unsigned)(base >> 32)})
    const int x = b & 7;
    if (w == 7) {
        u64 pc;
        asm volatile("s_getpc_b64 %0" : "=s"(pc));
        const char* cb = (const char*)(pc & ~63ull);
        const int chunk = (b >> 3);
        glds16(cb + (size_t)min(chunk * 2048 + lane * 16, CODE_PF - 16), (unsigned)(size_t)(L + L_JUNK));
        glds16(cb + (size_t)min(chunk * 2048 + 1024 + lane * 16, CODE_PF - 16), (unsigned)(size_t)(L + L_JUNK));
    }

    const unsigned tag32 = 0;
    (void)tag32;
#define TAG32 ((unsigned)(base >> 16) ^ 0x5bd1e995u)
    auto stats1 = [&](const v4u (&xr)[7]) {
        float sum = 0.f;
#pragma unroll
        for (int i = 0; i < 7; ++i)
#pragma unroll
            for (int j = 0; j < 4; ++j) sum += lo2f(xr[i][j]) + hi2f(xr[i][j]);
        sum += __shfl_xor(sum, 1); sum += __shfl_xor(sum, 2); sum += __shfl_xor(sum, 4);
        const float m = sum * (1.f / D);
        float var = 0.f;
#pragma unroll
        for (int i = 0; i < 7; ++i)
#pragma unroll
            for (int j = 0; j < 4; ++j) { float a = lo2f(xr[i][j]) - m, c = hi2f(xr[i][j]) - m; var = fmaf(a, a, var); var = fmaf(c, c, var); }
        var += __shfl_xor(var, 1); var += __shfl_xor(var, 2); var += __shfl_xor(var, 4);
        const float sd = __builtin_amdgcn_sqrtf(rbf(rbf(var * (1.f / D)) + 1e-5f));
        if ((lane & 7) == 0) __builtin_amdgcn_raw_buffer_store_b32(pk(m, sd), rs, O_ST1 + (8 * b + (lane >> 3)) * 4, 0, SC1);
    };
    const rsrc_t rq = __builtin_amdgcn_make_buffer_rsrc((void*)p.q, 0, S * D * 2, 0x00020000);

    if (b < NWORK) {
        const int g = b >> 5, rt = b & 31, idx = b >> 3;
        const int nt1 = 7 * (x >> 1) + idx % 7, pn1 = 4 * (x & 1) + idx / 7;
        const int rt2 = idx / 7, nt2 = idx % 7;
        const int cl = w >> 2, i0b = 2 * (w & 3);
        if (w < 4) {
            unsigned kk[11], vv[8];
            const int pp = tid & 31, rg = tid >> 5;
            const int row0 = 64 * rt + 8 * rg, col = 32 * g + pp;
            const rsrc_t rk = __builtin_amdgcn_make_buffer_rsrc((void*)p.k, 0, S * D * 2, 0x00020000);
            const rsrc_t rv = __builtin_amdgcn_make_buffer_rsrc((void*)p.v, 0, S * D * 2, 0x00020000);
            const int o0 = (row0 * NP + col) * 4;
            const int oh = row0 ? o0 - 3 * 896 : 0x40000000;
#pragma unroll
            for (int i = 0; i < 3; ++i) kk[i] = __builtin_amdgcn_raw_buffer_load_b32(rk, oh + i * 896, 0, AUXIN);
#pragma unroll
            for (int i = 0; i < 8; ++i) kk[3 + i] = __builtin_amdgcn_raw_buffer_load_b32(rk, o0 + i * 896, 0, AUXIN);
#pragma unroll
            for (int i = 0; i < 8; ++i) vv[i] = __builtin_amdgcn_raw_buffer_load_b32(rv, o0 + i * 896, 0, AUXIN);
            const v4u sh = *(const v4u*)(p.shk + col * 8);
            const v4u kf0 = LDIN((const v4u*)(p.ssmk + (size_t)2 * b * S + tid * 8)), kf1 = LDIN((const v4u*)(p.ssmk + (size_t)(2 * b + 1) * S + tid * 8));
            __builtin_amdgcn_sched_barrier(0);
            asm volatile("s_barrier" ::: "memory");
            GET_EPOCH();
            __builtin_amdgcn_sched_barrier(0);
            float s0[4] = {lo2f(sh.x), hi2f(sh.x), lo2f(sh.y), hi2f(sh.y)};
            float s1[4] = {lo2f(sh.z), hi2f(sh.z), lo2f(sh.w), hi2f(sh.w)};
            unsigned o[8];
#pragma unroll
            for (int i = 0; i < 8; ++i) {
                float a0, a1;
                a0 = lo2f(kk[i]) * s0[3]; a1 = hi2f(kk[i]) * s1[3];
#pragma unroll
                for (int t = 1; t < 4; ++t) { a0 = fmaf(lo2f(kk[i + t]), s0[3 - t], a0); a1 = fmaf(hi2f(kk[i + t]), s1[3 - t], a1); }
                o[i] = pk(a0 * lo2f(vv[i]), a1 * hi2f(vv[i]));
            }
            const int la = L_EX + pp * 256;
            const int sw0 = ((2 * rg) ^ (pp & 15)) * 16;
            *(LAS v4u*)(L + la + sw0) = v4u{o[0], o[1], o[2], o[3]};
            *(LAS v4u*)(L + la + (sw0 ^ 16)) = v4u{o[4], o[5], o[6], o[7]};
            lds_arrive(CNT(0), lane);
            lds_wait(CNT(0), 4);
            STAMP(1);
#pragma unroll
            for (int i = 0; i < 2; ++i) {
                const int p2 = 8 * w + 4 * i + (lane >> 4), sl = lane & 15;
                v4u a = *(LAS v4u*)(L + L_EX + p2 * 256 + ((sl ^ (p2 & 15)) * 16));
                st128(rs, O_KVP + (32 * g + p2) * (S * 4) + (64 * rt + 4 * sl) * 4, a);
            }
            if (rt == 0 && lane < 32) st64(rs, O_CB + ((lane * 7 + g) * 4 + w) * 64, basev);
            if (b == 0 && w == 0) st64(rs, O_CC + lane * 128, basev);
            if (w == 2 && lane < 10 && b * 10 + lane < S) st64(rs, O_ST2 + (b * 10 + lane) * 8, v2u{0u, ~TAG32});
            *(LAS v4u*)(L + L_KL + 32 + tid * 16) = kf0;
            *(LAS v4u*)(L + L_KL + KST + 32 + tid * 16) = kf1;
            lds_arrive(CNT(1), lane);
            DRAIN();
            if (lane == 0) st64(rs, O_FA + (w * NWORK + b) * 64, basev);
            STAMP(2);
        } else if (w < 6) {
            unsigned xx[16];
            const int pp = tid & 31, rg = (tid >> 5) & 3;
            const int o0 = ((64 * rt + 16 * rg) * NP + 32 * g + pp) * 4;
#pragma unroll
            for (int i = 0; i < 16; ++i) xx[i] = __builtin_amdgcn_raw_buffer_load_b32(rq, o0 + i * 896, 0, 0);
            v4u xr[14]; unsigned cv[14];
            const int slab = b >> 2, ks = b & 3;
            if (w == 4) {
#pragma unroll
                for (int i = 0; i < 7; ++i) xr[i] = __builtin_amdgcn_raw_buffer_load_b128(rq, (8 * b + (lane >> 3)) * 896 + ((lane & 7) + 8 * i) * 16, 0, 0);
                *(LAS v4u*)(L + L_U + (lane >> 5) * UST + 4096 + (lane & 31) * 16) = v4u{0, 0, 0, 0};
                if (lane < 4) *(LAS v4u*)(L + L_KL + (lane >> 1) * KST + (lane & 1) * 16) = v4u{0, 0, 0, 0};
                if (lane < 4) *(LAS v4u*)(L + L_MISC + lane * 16) = v4u{0, 0, 0, 0};
            } else {
                const rsrc_t rm = __builtin_amdgcn_make_buffer_rsrc((void*)p.wmod, 0, b < 168 ? D * 6 * D * 2 : 0, 0x00020000);
                const rsrc_t rc = __builtin_amdgcn_make_buffer_rsrc((void*)p.c, 0, D * 2, 0x00020000);
#pragma unroll
                for (int i = 0; i < 14; ++i) {
                    xr[i] = __builtin_amdgcn_raw_buffer_load_b128(rm, (112 * ks + 8 * i + (lane >> 3)) * (6 * D * 2) + slab * 128 + (lane & 7) * 16, 0, AUXIN);
                    cv[i] = __builtin_amdgcn_raw_buffer_load_b16(rc, (112 * ks + 8 * i + (lane >> 3)) * 2, 0, 0);
                }
            }
            __builtin_amdgcn_sched_barrier(0);
            BAR();
            GET_EPOCH();
            __builtin_amdgcn_sched_barrier(0);
#pragma unroll
            for (int i = 0; i < 4; ++i)
                *(LAS v4u*)(L + L_EX + 8192 + pp * 256 + (((4 * rg + i) ^ (pp & 15)) * 16)) = v4u{xx[4 * i], xx[4 * i + 1], xx[4 * i + 2], xx[4 * i + 3]};
            lds_arrive(CNT(2), lane);
            if (w == 4) {
                stats1((const v4u(&)[7])xr);
            } else {
                float a[8];
#pragma unroll
                for (int j = 0; j < 8; ++j) a[j] = 0.f;
#pragma unroll
                for (int i = 0; i < 14; ++i) {
                    const float cf = lo2f(cv[i]);
                    const float sc = rbf(cf * __builtin_amdgcn_rcpf(1.f + __expf(-cf)));
#pragma unroll
                    for (int j = 0; j < 4; ++j) { a[2 * j] = fmaf(sc, lo2f(xr[i][j]), a[2 * j]); a[2 * j + 1] = fmaf(sc, hi2f(xr[i][j]), a[2 * j + 1]); }
                }
                const bool b5 = lane & 32, b4 = lane & 16, b3 = lane & 8;
                float c4[4], c2[2];
#pragma unroll
                for (int j = 0; j < 4; ++j) { float keep = b5 ? a[4 + j] : a[j], send = b5 ? a[j] : a[4 + j]; c4[j] = keep + __shfl_xor(send, 32); }
#pragma unroll
                for (int j = 0; j < 2; ++j) { float keep = b4 ? c4[2 + j] : c4[j], send = b4 ? c4[j] : c4[2 + j]; c2[j] = keep + __shfl_xor(send, 16); }
                const float c1 = (b3 ? c2[1] : c2[0]) + __shfl_xor(b3 ? c2[0] : c2[1], 8);
                const int colm = slab * 64 + (lane & 7) * 8 + (lane >> 3);
                if (b < 168) __builtin_amdgcn_raw_buffer_store_b32(__float_as_uint(c1), rs, O_MODP + colm * 16 + ks * 4, 0, SC1);
            }
            lds_wait(CNT(2), 2);
            STAMP(1);
#pragma unroll
            for (int i = 0; i < 4; ++i) {
                const int p2 = 16 * (w - 4) + 4 * i + (lane >> 4), sl = lane & 15;
                v4u a = *(LAS v4u*)(L + L_EX + 8192 + p2 * 256 + ((sl ^ (p2 & 15)) * 16));
                st128(rs, O_XP + (32 * g + p2) * (S * 4) + (64 * rt + 4 * sl) * 4, a);
            }
            DRAIN();
            if (lane == 0) st64(rs, O_FX + ((w - 4) * 256 + b) * 64, basev);
            STAMP(2);
        } else {
            asm volatile("s_barrier" ::: "memory");
            const int r8 = lane >> 3, ch = lane & 7;
            const int mat = w - 6;
            const u16* wsrc = (mat ? p.wu : p.wg) + (size_t)r8 * F + nt1 * 64 + ((ch ^ (((r8 >> 1) & 1) << 2)) * 8);
            int i = (idx / 7) * 14;
#pragma unroll 2
            for (int n = 0; n < NW1A; ++n) {
                glds16(wsrc + (size_t)i * 8 * F, (unsigned)(size_t)(L + L_W1 + (i >> 1) * 4096 + mat * 2048 + (i & 1) * 1024));
                i = i == 55 ? 0 : i + 1;
            }
            STAMP(1);
            GET_EPOCH();
        }
        const int la_ = lane & 15, q_ = lane >> 4;
        lds_wait(CNT(1), 4);
        v4u KF[2][8];
        {
            const int kb = L_KL + cl * KST + ((2 + 2 * la_ + 16 * q_) & ~3) + 64 * i0b;
            const int shf = (la_ & 1) ? 0 : 16;
#pragma unroll
            for (int ii = 0; ii < 2; ++ii)
#pragma unroll
                for (int sg = 0; sg < 8; ++sg) {
                    unsigned d[5];
#pragma unroll
                    for (int j = 0; j < 5; ++j) d[j] = *(LAS unsigned*)(L + kb + ii * 64 + sg * 512 + j * 4);
#pragma unroll
                    for (int j = 0; j < 4; ++j) KF[ii][sg][j] = __builtin_amdgcn_alignbit(d[j + 1], d[j], shf);
                    asm volatile("" : "+v"(KF[ii][sg]));
                }
        }
        STAMP(3);
        if (w == 0) wait_words(rs, O_FA + (((b >> 3) & 3) * NWORK + 32 * g + (lane & 31)) * 64, base, p.ws, 1);
        BAR();
        STAMP(4);
        const int s0 = 256 * w + 16 * la_ + 4 * q_;
        int fxo;
        {
            int f;
            if (lane < 32) f = 32 * w + lane;
            else if (lane < 36) f = ((b >> 4) & 1) * 256 + 32 * g + 4 * w + (lane - 32);
            else if (lane < 52) { const int vi = (lane - 36) >> 2; f = 256 + (((vi == 3 ? 4 : vi) * 7 + g) * 4 + (lane & 3)); }
            else { const int sbx = w < 7 ? ((lane & 4) ? 21 + w : 28 + w) : 35 + nt2; f = 256 + sbx * 4 + (lane & 3); }
            fxo = O_FX + f * 64;
        }
        v4u kv4;
        if (w < 4) {
            kv4 = ld128(rs, O_KVP + b * (S * 4) + s0 * 4);
            v4u kvb = ld128(rs, O_KVP + b * (S * 4) + (s0 + 1024) * 4);
            const int ua = L_U + (2044 - s0) * 2;
            *(LAS v2u*)(L + ua) = v2u{__builtin_amdgcn_perm(kv4.z, kv4.w, 0x05040100), __builtin_amdgcn_perm(kv4.x, kv4.y, 0x05040100)};
            *(LAS v2u*)(L + ua + UST) = v2u{__builtin_amdgcn_perm(kv4.z, kv4.w, 0x07060302), __builtin_amdgcn_perm(kv4.x, kv4.y, 0x07060302)};
            *(LAS v2u*)(L + ua - 2048) = v2u{__builtin_amdgcn_perm(kvb.z, kvb.w, 0x05040100), __builtin_amdgcn_perm(kvb.x, kvb.y, 0x05040100)};
            *(LAS v2u*)(L + ua - 2048 + UST) = v2u{__builtin_amdgcn_perm(kvb.z, kvb.w, 0x07060302), __builtin_amdgcn_perm(kvb.x, kvb.y, 0x07060302)};
        }
        wait_words(rs, fxo, base, p.ws, 2);
        const v4u st1 = ld128(rs, O_ST1 + s0 * 4);
        const v4u x4 = ld128(rs, O_XP + b * (S * 4) + s0 * 4);
        if (w >= 4) kv4 = ld128(rs, O_KVP + b * (S * 4) + s0 * 4);
        const int vi_ = (lane >> 1) & 3;
        const v4u mp = ld128(rs, O_MODP + ((vi_ == 3 ? 4 : vi_) * D + 2 * b + (lane & 1)) * 16);
        const v4u cs0 = ld128(rs, O_MODP + ((w < 7 ? 21 + w : 35 + nt2) * 64 + lane) * 16);
        const v4u cs1 = ld128(rs, O_MODP + ((28 + w) * 64 + lane) * 16);
        const unsigned m_D = ((const unsigned*)p.ssmD)[b];
        __builtin_amdgcn_sched_barrier(0);
        BAR();
        STAMP(5);
        if (NW1A < 56 && w >= 6) {
            const int r8 = lane >> 3, ch = lane & 7;
            const int mat = w - 6;
            const u16* wsrc = (mat ? p.wu : p.wg) + (size_t)r8 * F + nt1 * 64 + ((ch ^ (((r8 >> 1) & 1) << 2)) * 8);
            int i = ((idx / 7) * 14 + NW1A) % 56;
#pragma unroll 2
            for (int n = NW1A; n < 56; ++n) {
                glds16(wsrc + (size_t)i * 8 * F, (unsigned)(size_t)(L + L_W1 + (i >> 1) * 4096 + mat * 2048 + (i & 1) * 1024));
                i = i == 55 ? 0 : i + 1;
            }
        }
        const int c31 = lane & 31, h_ = lane >> 5;
        unsigned WD[14][8];
        const rsrc_t rwd = __builtin_amdgcn_make_buffer_rsrc((void*)p.wd, 0, F * D * 2, 0x00020000);
        const int wdo = ((224 * w + 4 * h_) * NP + nt2 * 32 + c31) * 4;
#define LDWD(kk2, j) __builtin_amdgcn_raw_buffer_load_b32(rwd, wdo + ((j) & 3) * 896 + (16 * (kk2) + 8 * ((j) >> 2)) * 896, 0, 0)
#pragma unroll
        for (int kk2 = 0; kk2 < WD1; ++kk2)
#pragma unroll
            for (int j = 0; j < 8; ++j) WD[kk2][j] = LDWD(kk2, j);
        __builtin_amdgcn_sched_barrier(0);
        STAMP(6);
        v4f acc[8];
#pragma unroll
        for (int t = 0; t < 8; ++t) acc[t] = v4f{0.f, 0.f, 0.f, 0.f};
        const int ub = L_U + cl * UST + (2032 - 16 * la_ + 8 * q_ + 32 * i0b) * 2;
        v4u UF[2][4];
#pragma unroll
        for (int ii = 0; ii < 2; ++ii)
#pragma unroll
            for (int r = 0; r < 4; ++r) UF[ii][r] = *(LAS v4u*)(L + ub + ii * 64 - r * 512);
        __builtin_amdgcn_sched_barrier(0);
#pragma unroll
        for (int ii = 0; ii < 2; ++ii)
#pragma unroll
            for (int r = 0; r < 4; ++r)
#pragma unroll
                for (int sg = 0; sg + r < 4; ++sg) acc[sg + r] = mfma16(KF[ii][sg], UF[ii][r], acc[sg + r]);
#pragma unroll
        for (int t = 0; t < 4; ++t) *(LAS v4f*)(L + L_EX + (w * 4 + t) * 1024 + lane * 16) = acc[t];
        BAR();
        STAMP(7);
        auto finalize = [&](int tl) {
            v4f y0 = {0.f, 0.f, 0.f, 0.f}, y1 = {0.f, 0.f, 0.f, 0.f};
#pragma unroll
            for (int ww = 0; ww < 4; ++ww) {
                y0 += *(LAS v4f*)(L + L_EX + (ww * 4 + tl) * 1024 + lane * 16);
                y1 += *(LAS v4f*)(L + L_EX + ((ww + 4) * 4 + tl) * 1024 + lane * 16);
            }
            float mv = rbf((__uint_as_float(mp.x) + __uint_as_float(mp.y)) + (__uint_as_float(mp.z) + __uint_as_float(mp.w)));
            if (vi_ & 1) mv = rbf(1.f + mv);
            const float sa0 = rdl(mv, 0), sa1 = rdl(mv, 1), ca0 = rdl(mv, 2), ca1 = rdl(mv, 3), ga0 = rdl(mv, 4), ga1 = rdl(mv, 5), cb0 = rdl(mv, 6), cb1 = rdl(mv, 7);
            unsigned oa[4], ox[4];
#pragma unroll
            for (int i = 0; i < 4; ++i) {
                float a0 = y0[i], a1 = y1[i];
                const float xa = lo2f(x4[i]), xb = hi2f(x4[i]);
                const float mean = lo2f(st1[i]), rstd = __builtin_amdgcn_rcpf(hi2f(st1[i]));
                a0 = fmaf(lo2f(kv4[i]), lo2f(m_D), a0); a1 = fmaf(hi2f(kv4[i]), hi2f(m_D), a1);
                float n0 = (xa - mean) * rstd, n1 = (xb - mean) * rstd;
                n0 = fmaf(n0, ca0, sa0); n1 = fmaf(n1, ca1, sa1);
                ox[i] = pk(fmaf(n0 * a0, ga0, xa), fmaf(n1 * a1, ga1, xb));
                oa[i] = pk(lo2f(ox[i]) * cb0, hi2f(ox[i]) * cb1);
            }
            const int go = ((8 * w + (la_ >> 1)) * NP + b) * 128 + (16 * (la_ & 1) + 4 * q_) * 4;
            st128(rs, O_AP + go, v4u{oa[0], oa[1], oa[2], oa[3]});
            st128(rs, O_X1P + go, v4u{ox[0], ox[1], ox[2], ox[3]});
        };
        auto publishB = [&]() {
            DRAIN();
            if (lane < 4) add_word(p.ws, O_CB + (((w * 4 + lane) * 7 + g) * 4 + ((b >> 3) & 3)) * 64);
        };
        if (w < 4) finalize(w);
        __builtin_amdgcn_sched_barrier(0);
        STAMP(8);
#pragma unroll
        for (int ii = 0; ii < 2; ++ii)
#pragma unroll
            for (int r = 0; r < 4; ++r)
#pragma unroll
                for (int sg = 4 - r; sg + r < 8; ++sg) acc[sg + r] = mfma16(KF[ii][sg], UF[ii][r], acc[sg + r]);
        __builtin_amdgcn_sched_barrier(0);
        if (w < 4) publishB();
        __builtin_amdgcn_sched_barrier(0);
#pragma unroll
        for (int ii = 0; ii < 2; ++ii)
#pragma unroll
            for (int r = 0; r < 4; ++r) UF[ii][r] = *(LAS v4u*)(L + ub + ii * 64 - (r + 4) * 512);
        __builtin_amdgcn_sched_barrier(0);
#pragma unroll
        for (int ii = 0; ii < 2; ++ii)
#pragma unroll
            for (int r = 0; r < 4; ++r)
#pragma unroll
                for (int sg = 0; sg + r < 4; ++sg) acc[4 + sg + r] = mfma16(KF[ii][sg], UF[ii][r], acc[4 + sg + r]);
        {
            float a = rbf((__uint_as_float(cs0.x) + __uint_as_float(cs0.y)) + (__uint_as_float(cs0.z) + __uint_as_float(cs0.w)));
            float c = 1.f + rbf((__uint_as_float(cs1.x) + __uint_as_float(cs1.y)) + (__uint_as_float(cs1.z) + __uint_as_float(cs1.w)));
            const unsigned ac = pk(a, c);
            *(LAS u16*)(L + (w < 7 ? L_CS + w * 128 : L_GB) + lane * 2) = (u16)ac;
            if (w < 7) *(LAS u16*)(L + L_CS + 896 + w * 128 + lane * 2) = (u16)(ac >> 16);
        }
        BAR();
#pragma unroll
        for (int t = 0; t < 4; ++t) *(LAS v4f*)(L + L_EX + (w * 4 + t) * 1024 + lane * 16) = acc[4 + t];
        if (w >= 6) DRAIN();
        BAR();
        STAMP(9);
        const int tr_e = (lane & 15) >> 2, tr_p = lane & 3, tr_ch = (lane >> 4) & 1;
        const int trb = L_W1 + (8 * h_ + tr_e) * 128 + 32 * tr_ch + 8 * tr_p;
        const int trx = ((tr_e >> 1) & 1) * 64;
        auto wfrag = [&](int tile, int k1) -> v4u {
            const int o = trb + k1 * 4096 + (tile >> 1) * 2048 + (((tile & 1) * 64) ^ trx);
            v4s a = __builtin_amdgcn_ds_read_tr16_b64_v4i16((LAS v4s*)(L + o));
            v4s c = __builtin_amdgcn_ds_read_tr16_b64_v4i16((LAS v4s*)(L + o + 512));
            v2u a2 = __builtin_bit_cast(v2u, a), c2 = __builtin_bit_cast(v2u, c);
            return v4u{a2.x, a2.y, c2.x, c2.y};
        };
        if (w >= 4) { finalize(w - 4); lds_arrive(CNT(11), lane); publishB(); }
        else {
            v16f uacc;
#pragma unroll
            for (int i = 0; i < 16; ++i) uacc[i] = 0.f;
            const int mo = L_CS + (c31 < 16 ? 896 : 0) + 16 * h_;
#pragma unroll 1
            for (int k0 = 0; k0 < 28; k0 += 7) {
                v4u wf[7], xf[7];
#pragma unroll
                for (int i = 0; i < 7; ++i) { wf[i] = wfrag(w, k0 + i); xf[i] = *(LAS v4u*)(L + mo + (k0 + i) * 32); }
                __builtin_amdgcn_sched_barrier(0);
#pragma unroll
                for (int i = 0; i < 7; ++i) uacc = mfma32(wf[i], xf[i], uacc);
            }
            if ((lane & 15) == 0) {
#pragma unroll
                for (int g4 = 0; g4 < 4; ++g4)
                    *(LAS v4f*)(L + L_UV + ((c31 >> 4) * 128 + w * 32 + 8 * g4 + 4 * h_) * 4) = v4f{uacc[4 * g4], uacc[4 * g4 + 1], uacc[4 * g4 + 2], uacc[4 * g4 + 3]};
            }
            lds_arrive(CNT(5), lane);
        }
        STAMP(10);
        const int sb1 = cl * 32 + 4 * pn1 + (w & 3);
        if ((w & 3) == 0) {
            const int l28 = lane < 28 ? lane : 0;
            wait_words(rs, O_CB + (((cl * 4 + (pn1 >> 1)) * 4 + (x >> 1)) * 28 + l28) * 64, base + 8, p.ws, 3);
            lds_arrive(CNT(3 + cl), lane);
        } else lds_wait(CNT(3 + cl), 1);
        STAMP(11);
        v16f ga[4];
#pragma unroll
        for (int t = 0; t < 4; ++t)
#pragma unroll
            for (int i = 0; i < 16; ++i) ga[t][i] = 0.f;
        v2u st2;
        {
            constexpr int WIN = 8;
            v4u XF[WIN];
            v4u WA[2][4];
            const int xo = O_AP + sb1 * (NP * 128) + h_ * 512 + c31 * 4;
            auto ldx = [&](int k1) -> v4u {
                return v4u{ld32(rs, xo + k1 * 1024), ld32(rs, xo + k1 * 1024 + 128), ld32(rs, xo + k1 * 1024 + 256), ld32(rs, xo + k1 * 1024 + 384)};
            };
#pragma unroll
            for (int i = 0; i < WIN; ++i) XF[i] = ldx(i);
#pragma unroll
            for (int t = 0; t < 4; ++t) WA[0][t] = wfrag(t, 0);
            __builtin_amdgcn_sched_barrier(0);
#pragma unroll
            for (int k1 = 0; k1 < 28; ++k1) {
                if (k1 + 1 < 28) {
#pragma unroll
                    for (int t = 0; t < 4; ++t) WA[(k1 + 1) & 1][t] = wfrag(t, k1 + 1);
                }
                __builtin_amdgcn_sched_barrier(0);
                const v4u xf = XF[k1 % WIN];
#pragma unroll
                for (int t = 0; t < 4; ++t) ga[t] = mfma32(WA[k1 & 1][t], xf, ga[t]);
                __builtin_amdgcn_sched_barrier(0);
                if (k1 == 0) STAMP(12);
                if (k1 + WIN < 28) XF[k1 % WIN] = ldx(k1 + WIN);
                if (k1 >= WDS && k1 - WDS < 2 * (14 - WD1)) {
                    const int kk2 = WD1 + ((k1 - WDS) >> 1), jh = ((k1 - WDS) & 1) * 4;
#pragma unroll
                    for (int j = 0; j < 4; ++j) WD[kk2][jh + j] = LDWD(kk2, jh + j);
                }
                if (k1 == 24) st2 = ld64(rs, O_ST2 + (sb1 * 32 + c31) * 8);
                __builtin_amdgcn_sched_barrier(0);
            }
        }
        lds_arrive(CNT(8), lane);
        STAMP(13);
        {
#pragma unroll 1
            for (int spins = 0; !__all(st2.y == TAG32); ++spins) {
                asm volatile("" ::: "memory");
                st2 = ld64(rs, O_ST2 + (sb1 * 32 + c31) * 8);
                if (spins > SPIN_MAX) { __builtin_amdgcn_raw_buffer_store_b32(4, rs, O_TMO, 0, SC1); break; }
            }
            lds_wait(CNT(5), 4);
            STAMP(14);
            const float r2 = __builtin_amdgcn_rcpf(hi2f(st2.x)), nmr = -lo2f(st2.x) * r2;
#pragma unroll
            for (int t = 0; t < 2; ++t) {
                unsigned o[8];
#pragma unroll
                for (int g4 = 0; g4 < 4; ++g4) {
                    const int uo = L_UV + (t * 32 + 8 * g4 + 4 * h_) * 4;
                    v4f u1g = *(LAS v4f*)(L + uo), u1u = *(LAS v4f*)(L + uo + 256), u2g = *(LAS v4f*)(L + uo + 512), u2u = *(LAS v4f*)(L + uo + 768);
                    float r[4];
#pragma unroll
                    for (int i = 0; i < 4; ++i) {
                        float gg = fmaf(r2, ga[t][4 * g4 + i], fmaf(nmr, u1g[i], u2g[i]));
                        float uu = fmaf(r2, ga[2 + t][4 * g4 + i], fmaf(nmr, u1u[i], u2u[i]));
                        r[i] = silu_f(gg) * uu;
                    }
                    o[2 * g4] = pk(r[0], r[1]); o[2 * g4 + 1] = pk(r[2], r[3]);
                }
                const int go = O_G + (sb1 * 112 + 4 * nt1 + 2 * t) * 1024 + lane * 16;
                st128(rs, go, v4u{o[0], o[1], o[2], o[3]});
                st128(rs, go + 1024, v4u{o[4], o[5], o[6], o[7]});
            }
            STAMP(15);
            DRAIN();
            if (lane == 0) add_word(p.ws, O_CC + sb1 * 128);
        }
        STAMP(16);
        v4u WF[14][2];
#pragma unroll
        for (int kk2 = 0; kk2 < 14; ++kk2) {
#pragma unroll
            for (int i = 0; i < 4; ++i) {
                WF[kk2][0][i] = __builtin_amdgcn_perm(WD[kk2][2 * i + 1], WD[kk2][2 * i], 0x05040100);
                WF[kk2][1][i] = __builtin_amdgcn_perm(WD[kk2][2 * i + 1], WD[kk2][2 * i], 0x07060302);
            }
            asm volatile("" : "+v"(WF[kk2][0]), "+v"(WF[kk2][1]));
        }
#pragma unroll
        for (int st = 0; st < 2; ++st) {
            const int sb2 = st * 32 + 4 * x + rt2;
            if (w == 4 * st) { wait_words(rs, O_CC + sb2 * 128, base + 28, p.ws, 5); lds_arrive(CNT(6 + st), lane); }
            else lds_wait(CNT(6 + st), 1);
            STAMP(17 + 3 * st);
            v4u GF[14];
            const int go = O_G + (sb2 * 112 + 14 * w) * 1024 + lane * 16;
#pragma unroll
            for (int i = 0; i < 14; ++i) GF[i] = ld128(rs, go + i * 1024);
            unsigned x1v[4];
            if (st == 0) {
#pragma unroll
                for (int i = 0; i < 4; ++i) x1v[i] = ld32(rs, O_X1P + (sb2 * NP + 32 * nt2 + 4 * (tid & 7) + i) * 128 + ((tid >> 3) & 31) * 4);
            } else {
#pragma unroll
                for (int i = 0; i < 2; ++i) x1v[i] = ld32(rs, O_X1P + (sb2 * NP + 32 * nt2 + 2 * (tid & 15) + i) * 128 + (tid >> 4) * 4);
            }
            __builtin_amdgcn_sched_barrier(0);
            v16f oa[2];
#pragma unroll
            for (int i = 0; i < 16; ++i) { oa[0][i] = 0.f; oa[1][i] = 0.f; }
#pragma unroll
            for (int kk2 = 0; kk2 < 14; ++kk2) {
                oa[0] = mfma32(WF[kk2][0], GF[kk2], oa[0]);
                oa[1] = mfma32(WF[kk2][1], GF[kk2], oa[1]);
            }
            STAMP(18 + 3 * st);
            const int po = st ? 32768 + w * 8192 : (w < 4 ? L_EX + w * 8192 : (w - 4) * 8192);
            if (st == 0 && w < 4) lds_wait(CNT(11), 4); else lds_wait(CNT(8), 8);
#pragma unroll
            for (int t = 0; t < 2; ++t)
#pragma unroll
                for (int g4 = 0; g4 < 4; ++g4)
                    *(LAS v4f*)(L + po + t * 4096 + c31 * 128 + (((2 * g4 + h_) ^ (c31 & 7)) * 16)) =
                        v4f{oa[t][4 * g4], oa[t][4 * g4 + 1], oa[t][4 * g4 + 2], oa[t][4 * g4 + 3]};
            lds_arrive(CNT(9 + st), lane);
            if (st == 0) {
                if (w < 4) {
                    const int sl = tid >> 3, cg = tid & 7;
                    lds_wait(CNT(9), 8);
                    v4f r0 = {0.f, 0.f, 0.f, 0.f}, r1 = {0.f, 0.f, 0.f, 0.f};
                    const int ro = sl * 128 + ((cg ^ (sl & 7)) * 16);
#pragma unroll
                    for (int ww = 0; ww < 4; ++ww) {
                        r0 += *(LAS v4f*)(L + L_EX + ww * 8192 + ro); r1 += *(LAS v4f*)(L + L_EX + ww * 8192 + 4096 + ro);
                        r0 += *(LAS v4f*)(L + ww * 8192 + ro); r1 += *(LAS v4f*)(L + ww * 8192 + 4096 + ro);
                    }
                    const v4u gbv = *(LAS v4u*)(L + L_GB + cg * 16);
                    unsigned o[4];
#pragma unroll
                    for (int i = 0; i < 4; ++i) {
                        float a0 = r0[i], a1 = r1[i]; rb2(a0, a1);
                        a0 *= lo2f(gbv[i]); a1 *= hi2f(gbv[i]); rb2(a0, a1);
                        o[i] = pk(lo2f(x1v[i]) + a0, hi2f(x1v[i]) + a1);
                    }
                    *(v4u*)(p.out + (size_t)(sb2 * 32 + sl) * D + 64 * nt2 + 8 * cg) = v4u{o[0], o[1], o[2], o[3]};
                }
            } else {
                const int sl = tid >> 4, cg = (tid >> 1) & 7, hf = tid & 1;
                lds_wait(CNT(10), 8);
                v2f r0 = {0.f, 0.f}, r1 = {0.f, 0.f};
                const int ro = 32768 + sl * 128 + ((cg ^ (sl & 7)) * 16) + hf * 8;
#pragma unroll
                for (int ww = 0; ww < 8; ++ww) { r0 += *(LAS v2f*)(L + ww * 8192 + ro); r1 += *(LAS v2f*)(L + ww * 8192 + 4096 + ro); }
                const v2u gbv = *(LAS v2u*)(L + L_GB + (tid & 15) * 8);
                unsigned o[2];
#pragma unroll
                for (int i = 0; i < 2; ++i) {
                    float a0 = r0[i], a1 = r1[i]; rb2(a0, a1);
                    a0 *= lo2f(gbv[i]); a1 *= hi2f(gbv[i]); rb2(a0, a1);
                    o[i] = pk(lo2f(x1v[i]) + a0, hi2f(x1v[i]) + a1);
                }
                *(v2u*)(p.out + (size_t)(sb2 * 32 + sl) * D + 64 * nt2 + 4 * (tid & 15)) = v2u{o[0], o[1]};
            }
            STAMP(19 + 3 * st);
        }
    } else {
        const int hid = b - NWORK, hi = hid >> 3;
        if (w == 4) {
            v4u xr[7];
#pragma unroll
            for (int i = 0; i < 7; ++i) xr[i] = __builtin_amdgcn_raw_buffer_load_b128(rq, (8 * b + (lane >> 3)) * 896 + ((lane & 7) + 8 * i) * 16, 0, 0);
            if (lane < 4) *(LAS v4u*)(L + L_MISC + lane * 16) = v4u{0, 0, 0, 0};
            __builtin_amdgcn_sched_barrier(0);
            GET_EPOCH();
            if (lane == 0) *(LAS v2u*)(L + 65536) = v2u{(unsigned)epoch, (unsigned)(epoch >> 32)};
            stats1(xr);
            DRAIN();
            if (lane == 0) st64(rs, O_FX + b * 64, basev);
            STAMP(2);
        } else {
        }
        BAR();
        {
            const v2u e2 = *(LAS v2u*)(L + 65536);
            epoch = (u64)(unsigned)__builtin_amdgcn_readfirstlane(e2.x) | ((u64)(unsigned)__builtin_amdgcn_readfirstlane(e2.y) << 32);
            base = (epoch << 16) + 0x9E3779B97F4A0000ull;
        }
        const int st = w >> 2, t4 = tid & 255;
        const int sb = st * 32 + 4 * x + hi;
        if ((w & 3) == 0) {
            const int l28 = lane < 28 ? lane : 0;
            wait_words(rs, O_CB + (((sb >> 3) * 4 + (x >> 1)) * 28 + l28) * 64, base + 8, p.ws, 6);
            lds_arrive(CNT(st), lane);
        } else lds_wait(CNT(st), 1);
        STAMP(11);
        const int sq = t4 & 7, pg = t4 >> 3;
        const int xo = O_X1P + sb * (NP * 128) + sq * 16;
        v4u xv[7];
#pragma unroll
        for (int i = 0; i < 7; ++i) xv[i] = ld128(rs, xo + (pg + 32 * i) * 128);
        const v4u pv = ld128(rs, xo);
        float s1[4], s2[4];
#pragma unroll
        for (int j = 0; j < 4; ++j) {
            const float piv = lo2f(pv[j]);
            s1[j] = 0.f; s2[j] = 0.f;
#pragma unroll
            for (int i = 0; i < 7; ++i) {
                float a = lo2f(xv[i][j]) - piv, c = hi2f(xv[i][j]) - piv;
                s1[j] += a + c; s2[j] = fmaf(a, a, fmaf(c, c, s2[j]));
            }
        }
        const int lo_ = st * 16384;
        *(LAS v4f*)(L + lo_ + pg * 144 + sq * 16) = v4f{s1[0], s1[1], s1[2], s1[3]};
        *(LAS v4f*)(L + lo_ + 4608 + pg * 144 + sq * 16) = v4f{s2[0], s2[1], s2[2], s2[3]};
        if (t4 < 8) *(LAS v4f*)(L + lo_ + 9216 + sq * 16) = v4f{lo2f(pv[0]), lo2f(pv[1]), lo2f(pv[2]), lo2f(pv[3])};
        lds_arrive(CNT(2 + st), lane);
        lds_wait(CNT(2 + st), 4);
        {
            const int rr = (w & 3) * 8 + (lane & 7), part = (lane >> 3) & 3, arr = lane >> 5;
            float a = 0.f;
#pragma unroll
            for (int i = 0; i < 8; ++i) a += *(LAS float*)(L + lo_ + arr * 4608 + (part * 8 + i) * 144 + rr * 4);
            a += __shfl_xor(a, 8); a += __shfl_xor(a, 16);
            const float c = __shfl_xor(a, 32);
            const float piv = *(LAS float*)(L + lo_ + 9216 + rr * 4);
            const float md = a * (1.f / D);
            const float var = fmaxf(c * (1.f / D) - md * md, 0.f);
            const float sd = __builtin_amdgcn_sqrtf(rbf(rbf(var) + 1e-5f));
            if (lane < 8) st64(rs, O_ST2 + (sb * 32 + rr) * 8, v2u{pk(piv + md, sd), TAG32});
        }
        STAMP(12);
        if (hid == 7 && tid == 256) {
            const u64 e = epoch + 1;
            st64(rs, O_EPOCH, v2u{(unsigned)e, (unsigned)(e >> 32)});
        }
    }
    STAMP_OUT;
}

extern "C" void kernel_launch(void* const* d_in, const int* in_sizes, int n_in,
                              void* d_out, int out_size, void* d_ws, size_t ws_size,
                              hipStream_t stream) {
    Params p;
    p.q = (const u16*)d_in[0]; p.k = (const u16*)d_in[1]; p.v = (const u16*)d_in[2];
    p.shk = (const u16*)d_in[3]; p.ssmk = (const u16*)d_in[4]; p.ssmD = (const u16*)d_in[5];
    p.wg = (const u16*)d_in[7]; p.wu = (const u16*)d_in[8]; p.wd = (const u16*)d_in[9];
    p.c = (const u16*)d_in[10]; p.wmod = (const u16*)d_in[11];
    p.out = (u16*)d_out; p.ws = (char*)d_ws;
    (void)hipFuncSetAttribute((const void*)fused, hipFuncAttributeMaxDynamicSharedMemorySize, L_TOTAL);
    fused<<<NWORK + NHELP, 512, L_TOTAL, stream>>>(p);
}
```

```cpp
#include <hip/hip_runtime.h>
#include <hip/hip_bf16.h>
#include <stdint.h>

#if defined(__HIP_DEVICE_COMPILE__)
#pragma clang attribute push (__attribute__((target("no-packed-fp32-ops"))), apply_to = function)
#endif
typedef unsigned short u16;
typedef unsigned long long u64;
typedef unsigned v4u __attribute__((ext_vector_type(4)));
typedef unsigned v2u __attribute__((ext_vector_type(2)));
typedef float v4f __attribute__((ext_vector_type(4)));
typedef float v2f __attribute__((ext_vector_type(2)));
typedef float v16f __attribute__((ext_vector_type(16)));
typedef short v4s __attribute__((ext_vector_type(4)));
typedef __bf16 v2bf __attribute__((ext_vector_type(2)));
typedef __bf16 v8bf __attribute__((ext_vector_type(8)));
#define LAS __attribute__((address_space(3)))

constexpr int S = 2048, D = 448, F = 1792, NP = 224;
constexpr int NWORK = 224, NHELP = 32;

constexpr int CODE_PF = 33792;
constexpr int PFTHR = 10, CSLP = 2, WLAG = 6, ASLP = 1, BSLP = 1, RD = 6, SWD = 8, ADEL = 14, XDEL = 44, XSLP = 3, HSL2 = 20, HSLP = 1;
constexpr int GW = 4, XWIN = 8, AUXIN = 0, AUXX = 0;

constexpr int O_EPOCH = 0;
constexpr int O_TMO = 1024;
constexpr int O_MODF = 4096;
constexpr int O_ST1 = 12288;
constexpr int O_ST2 = 28672;
constexpr int O_MODP = 45056;
constexpr int O_FB = 204800;
constexpr int O_RB = 10240;
constexpr int O_FC = 262144;
constexpr int O_KVP = 393216;
constexpr int KVPL = NP * S * 4;
constexpr int O_XP = O_KVP + 2 * KVPL;
constexpr int O_AP = O_XP + 2 * KVPL;
constexpr int O_X1P = O_AP + NP * S * 4;
constexpr int O_G = O_X1P + NP * S * 4;
constexpr int O_END = O_G + S * F * 2;

constexpr int L_W1 = 0;
constexpr int L_U = 114688;
constexpr int UST = 4608;
constexpr int L_EX = L_U + 2 * UST;
constexpr int L_KL = L_EX + 16384;
constexpr int KST = 4160;
constexpr int L_MS = 0;
constexpr int L_XT = 16384;
constexpr int L_X1A = L_U + 4096;
constexpr int L_X1B = 98304;
constexpr int L_CS = L_EX + 32768;
constexpr int L_GB = L_CS + 1792;
constexpr int L_MISC = L_GB + 128;
constexpr int L_JUNK = L_MISC + 64;
constexpr int L_UV = L_JUNK + 1024;
constexpr int L_TOTAL = L_UV + 2048;
static_assert(L_TOTAL <= 163840, "lds");
#define CNT(i) (L_MISC + 4 * (i))

constexpr int SC1 = 16;
constexpr int SPIN_MAX = 1 << 18;

struct Params {
    const u16 *q, *k, *v, *shk, *ssmk, *ssmD, *wg, *wu, *wd, *c, *wmod;
    u16* out;
    char* ws;
};

__device__ __forceinline__ float u2f(unsigned x) { return __builtin_bit_cast(float, x); }
__device__ __forceinline__ unsigned f2u(float x) { return __builtin_bit_cast(unsigned, x); }
__device__ __forceinline__ bool wall(bool c) { return __builtin_amdgcn_ballot_w64(!c) == 0; }
__device__ __forceinline__ float lo2f(unsigned x) { return u2f(x << 16); }
__device__ __forceinline__ float hi2f(unsigned x) { return u2f(x & 0xffff0000u); }
__device__ __forceinline__ unsigned pk(float a, float b) {
    v2f x = {a, b};
    return __builtin_bit_cast(unsigned, __builtin_convertvector(x, v2bf));
}
__device__ __forceinline__ float rbf(float a) { return lo2f(pk(a, a)); }
__device__ __forceinline__ void rb2(float& a, float& b) { unsigned p = pk(a, b); a = lo2f(p); b = hi2f(p); }

#define BAR() asm volatile("s_waitcnt lgkmcnt(0)\n\ts_barrier" ::: "memory")
#define DRAIN() asm volatile("s_waitcnt vmcnt(0)" ::: "memory")
#define SCHED() __builtin_amdgcn_sched_barrier(0)
#define PIN(x) asm volatile("" : "+v"(x))

#define FENCE() do { SCHED(); asm volatile("" ::: "memory"); SCHED(); } while (0)

__device__ __forceinline__ v4f mfma16(v4u a, v4u b, v4f c) {
    return __builtin_amdgcn_mfma_f32_16x16x32_bf16(__builtin_bit_cast(v8bf, a), __builtin_bit_cast(v8bf, b), c, 0, 0, 0);
}
__device__ __forceinline__ v16f mfma32(v4u a, v4u b, v16f c) {
    return __builtin_amdgcn_mfma_f32_32x32x16_bf16(__builtin_bit_cast(v8bf, a), __builtin_bit_cast(v8bf, b), c, 0, 0, 0);
}

template <int V> struct IC { static constexpr int value = V; };
typedef __amdgpu_buffer_rsrc_t rsrc_t;
__device__ __forceinline__ v4u ld128(rsrc_t r, int off) { return __builtin_amdgcn_raw_buffer_load_b128(r, off, 0, SC1); }
__device__ __forceinline__ v2u ld64(rsrc_t r, int off) { return __builtin_amdgcn_raw_buffer_load_b64(r, off, 0, SC1); }
__device__ __forceinline__ unsigned ld32(rsrc_t r, int off) { return __builtin_amdgcn_raw_buffer_load_b32(r, off, 0, SC1); }
__device__ __forceinline__ void st128(rsrc_t r, int off, v4u v) { __builtin_amdgcn_raw_buffer_store_b128(v, r, off, 0, SC1); }
__device__ __forceinline__ void st64(rsrc_t r, int off, v2u v) { __builtin_amdgcn_raw_buffer_store_b64(v, r, off, 0, SC1); }
constexpr int OOB = 0x40000000;

template <int SLP = 0>
__device__ __forceinline__ void wait_words(rsrc_t r, int off, u64 want, unsigned code) {
#pragma unroll 1
    for (int spins = 0;; ++spins) {
        asm volatile("" ::: "memory");
        const v2u x = ld64(r, off);
        if (wall(((x.x ^ (unsigned)want) | (x.y ^ (unsigned)(want >> 32))) == 0)) break;
        if (SLP) __builtin_amdgcn_s_sleep(SLP);
        if (spins > SPIN_MAX) { __builtin_amdgcn_raw_buffer_store_b32(code, r, O_TMO, 0, SC1); break; }
    }
    __builtin_amdgcn_fence(__ATOMIC_ACQUIRE, "wavefront");
    asm volatile("" ::: "memory");
}
__device__ __forceinline__ void glds16(const void* gsrc, unsigned lds_dst) {
    unsigned keep;
    asm volatile("s_mov_b32 %0, m0\n\ts_mov_b32 m0, %2\n\ts_nop 0\n\tglobal_load_lds_dwordx4 %1, off\n\ts_mov_b32 m0, %0"
                 : "=&s"(keep) : "v"(gsrc), "s"(lds_dst) : "memory");
}
template <int THR, int CNTK>
__device__ __forceinline__ void w1_unit(const u16* mat, unsigned v0, unsigned lds) {
    unsigned keep, cnt = CNTK, v1 = v0 + 8 * F * 2 - 1024;
    asm volatile(
        "s_mov_b32 %0, m0\n"
        "1:\n\t"
        "s_waitcnt vmcnt(%6)\n\t"
        "s_mov_b32 m0, %1\n\t"
        "s_add_u32 %1, %1, 0x1000\n\t"
        "global_load_lds_dwordx4 %3, %5\n\t"
        "global_load_lds_dwordx4 %4, %5 offset:1024\n\t"
        "v_add_u32 %3, 0xe000, %3\n\t"
        "v_add_u32 %4, 0xe000, %4\n\t"
        "s_sub_u32 %2, %2, 1\n\t"
        "s_cmp_lg_u32 %2, 0\n\t"
        "s_cbranch_scc1 1b\n\t"
        "s_mov_b32 m0, %0"
        : "=&s"(keep), "+s"(lds), "+s"(cnt), "+v"(v0), "+v"(v1) : "s"(mat), "i"(THR) : "memory", "scc");
}
template <int THR>
__device__ __forceinline__ void pf_stream(const u16* src, unsigned v0, unsigned stride, unsigned cnt, unsigned lds) {
    unsigned keep;
    asm volatile(
        "s_mov_b32 %0, m0\n\t"
        "s_mov_b32 m0, %5\n"
        "1:\n\t"
        "s_waitcnt vmcnt(%6)\n\t"
        "global_load_lds_dwordx4 %2, %3\n\t"
        "v_add_u32 %2, %4, %2\n\t"
        "s_sub_u32 %1, %1, 1\n\t"
        "s_cmp_lg_u32 %1, 0\n\t"
        "s_cbranch_scc1 1b\n\t"
        "s_mov_b32 m0, %0"
        : "=&s"(keep), "+s"(cnt), "+v"(v0) : "s"(src), "s"(stride), "s"(lds), "i"(THR) : "memory", "scc");
}
__device__ __forceinline__ void lds_arrive(unsigned addr) {
    asm volatile("s_waitcnt lgkmcnt(0)\n\ts_mov_b64 exec, 1\n\tds_add_u32 %0, %1\n\ts_mov_b64 exec, -1" :: "v"(addr), "v"(1u) : "memory");
}
__device__ __forceinline__ void lds_wait(LAS char* L, int off, unsigned n) {
#pragma unroll 1
    while (__builtin_amdgcn_readfirstlane(*(volatile LAS unsigned*)(L + off)) < n) {}
    asm volatile("" ::: "memory");
}
__device__ __forceinline__ v4u trfrag(LAS char* L, int o) {
    v4s a = __builtin_amdgcn_ds_read_tr16_b64_v4i16((LAS v4s*)(L + o));
    v4s c = __builtin_amdgcn_ds_read_tr16_b64_v4i16((LAS v4s*)(L + o + 512));
    v2u a2 = __builtin_bit_cast(v2u, a), c2 = __builtin_bit_cast(v2u, c);
    return v4u{a2.x, a2.y, c2.x, c2.y};
}
__device__ __forceinline__ float dot2(unsigned a, unsigned b, float c) {
    return __builtin_amdgcn_fdot2_f32_bf16(__builtin_bit_cast(v2bf, a), __builtin_bit_cast(v2bf, b), c, false);
}
template <int CTRL> __device__ __forceinline__ float dppx(float v) {
    return u2f(__builtin_amdgcn_update_dpp(0u, f2u(v), CTRL, 0xf, 0xf, true));
}
__device__ __forceinline__ float rdl(float v, int l) { return u2f(__builtin_amdgcn_readlane(f2u(v), l)); }
__device__ __forceinline__ float silu_f(float g) { return g * __builtin_amdgcn_rcpf(1.f + __expf(-g)); }

__global__ __launch_bounds__(512, 2) void fused(Params p) {
    extern __shared__ __attribute__((aligned(16))) char smem[];
    LAS char* L = (LAS char*)smem;
    const unsigned LB = (unsigned)(size_t)L;
    const int b = blockIdx.x, tid = threadIdx.x, lane = tid & 63;
    const int w = __builtin_amdgcn_readfirstlane(tid >> 6);
    const rsrc_t rs = __builtin_amdgcn_make_buffer_rsrc(p.ws, 0, O_END, 0x00020000);
    FENCE();
    u64 epoch, base;
    unsigned m_D;
    const u16* dptr = p.ssmD + 2 * (b < NWORK ? b : 0);
#define ISSUE_EPOCH() asm volatile("s_load_dwordx2 %0, %2, 0x0\n\ts_load_dword %1, %3, 0x0" : "=&s"(epoch), "=&s"(m_D) : "s"(p.ws), "s"(dptr) : "memory")
#define GET_EPOCH() do { asm volatile("s_waitcnt lgkmcnt(0)" : "+s"(epoch), "+s"(m_D) :: "memory"); base = (epoch << 16) + 0x9E3779B97F4A0000ull; } while (0)
#define basev (v2u{(unsigned)base, (unsigned)(base >> 32)})
#define TAG32 ((unsigned)(base >> 16) ^ 0x5bd1e995u)
    const int x = b & 7;
    u64 pc0;
    asm volatile("s_getpc_b64 %0" : "=s"(pc0));
    auto code_pf = [&]() {
        const char* cb = (const char*)(pc0 & ~63ull);
        glds16(cb + (size_t)min((b >> 3) * 1024 + lane * 16, CODE_PF - 16), LB + L_JUNK);
        if (CODE_PF > 32768 && b < 32) glds16(cb + (size_t)min(32768 + (b >> 3) * 1024 + lane * 16, CODE_PF - 16), LB + L_JUNK);
    };
    auto stats1 = [&](const v4u (&xr)[7]) {
        float s1[4] = {0.f, 0.f, 0.f, 0.f}, s2[4] = {0.f, 0.f, 0.f, 0.f};
#pragma unroll
        for (int i = 0; i < 7; ++i)
#pragma unroll
            for (int j = 0; j < 4; ++j) { s1[j] = dot2(xr[i][j], 0x3f803f80u, s1[j]); s2[j] = dot2(xr[i][j], xr[i][j], s2[j]); }
        float a = (s1[0] + s1[1]) + (s1[2] + s1[3]), c = (s2[0] + s2[1]) + (s2[2] + s2[3]);
        a += dppx<0xB1>(a); c += dppx<0xB1>(c); a += dppx<0x4E>(a); c += dppx<0x4E>(c); a += dppx<0x141>(a); c += dppx<0x141>(c);
        const float m = a * (1.f / D);
        const float var = fmaxf(c * (1.f / D) - m * m, 0.f);
        const float sd = __builtin_amdgcn_sqrtf(rbf(rbf(var) + 1e-5f));
        st64(rs, (lane & 7) == 0 ? O_ST1 + (8 * b + (lane >> 3)) * 8 : OOB, v2u{pk(m, sd), TAG32});
    };
    const rsrc_t rq = __builtin_amdgcn_make_buffer_rsrc((void*)p.q, 0, S * D * 2, 0x00020000);
    auto mod_load = [&](v4u (&wm)[8], unsigned& cv, int slab, int ks, bool on) {
        const rsrc_t rm = __builtin_amdgcn_make_buffer_rsrc((void*)p.wmod, 0, on ? D * 6 * D * 2 : 0, 0x00020000);
        cv = ((const unsigned*)p.c)[32 * ks + (lane & 31)];
#pragma unroll
        for (int i = 0; i < 8; ++i) wm[i] = __builtin_amdgcn_raw_buffer_load_b128(rm, (64 * ks + 8 * i + (lane >> 3)) * (6 * D * 2) + slab * 128 + (lane & 7) * 16, 0, AUXIN);
    };
    auto mod_unit = [&](const v4u (&wm)[8], unsigned cv, int ms) -> float {
        const int r8 = lane >> 3, te = (lane & 15) >> 2, tx = ((te >> 1) & 1) * 64;
        *(LAS unsigned*)(L + ms + lane * 4) = pk(silu_f(lo2f(cv)), silu_f(hi2f(cv)));
        v16f m0, m1;
#pragma unroll
        for (int i = 0; i < 16; ++i) { m0[i] = 0.f; m1[i] = 0.f; }
        const int wo = ms + 256 + r8 * 128 + (((lane & 7) ^ (((r8 >> 1) & 1) << 2)) * 16);
        const int ro = ms + 256 + (8 * (lane >> 5) + te) * 128 + 32 * ((lane >> 4) & 1) + 8 * (lane & 3);
#pragma unroll
        for (int i = 0; i < 8; ++i) *(LAS v4u*)(L + wo + i * 1024) = wm[i];
#pragma unroll
        for (int i = 0; i < 4; ++i) {
            const v4u sf = *(LAS v4u*)(L + ms + i * 32 + (lane >> 5) * 16);
            m0 = mfma32(sf, trfrag(L, ro + i * 2048 + tx), m0);
            m1 = mfma32(sf, trfrag(L, ro + i * 2048 + (64 ^ tx)), m1);
        }
        return lane < 32 ? m0[0] : m1[0];
    };

    if (b >= NWORK) {
        const int hid = b - NWORK, hi = hid >> 3;
        if (w == 0) {
            v4u xr[7];
#pragma unroll
            for (int i = 0; i < 7; ++i) xr[i] = __builtin_amdgcn_raw_buffer_load_b128(rq, (8 * b + (lane >> 3)) * 896 + ((lane & 7) + 8 * i) * 16, 0, AUXX);
            if (lane < 4) *(LAS v4u*)(L + L_MISC + lane * 16) = v4u{0, 0, 0, 0};
            SCHED();
            ISSUE_EPOCH();
            GET_EPOCH();
            if (lane == 0) *(LAS v2u*)(L + 65536) = v2u{(unsigned)epoch, (unsigned)(epoch >> 32)};
            stats1(xr);
            FENCE();
            BAR();
        } else {
            if (w == 7) code_pf();
            v4u wm[8]; unsigned cv;
            __builtin_amdgcn_s_sleep(HSL2);
            mod_load(wm, cv, hid < 7 ? 21 + hid : 28 + hid, w - 1, hid < 14);
            *(LAS float*)(L + 73728 + w * 256 + lane * 4) = mod_unit(wm, cv, (w - 1) * 8704);
            BAR();
            if (w >= 4) {
                FENCE();
                pf_stream<PFTHR>(p.wd, hid * 50176 + (w - 4) * 12544 + (lane < 49 ? lane : 48) * 16, 784, 16, LB + L_JUNK);
                FENCE();
            }
        }
        if (w) {
            const v2u e2 = *(LAS v2u*)(L + 65536);
            epoch = (u64)(unsigned)__builtin_amdgcn_readfirstlane(e2.x) | ((u64)(unsigned)__builtin_amdgcn_readfirstlane(e2.y) << 32);
            base = (epoch << 16) + 0x9E3779B97F4A0000ull;
        }
        if (w >= 4) {
            if (hi < 2) {
                wait_words<HSLP>(rs, O_FB + ((hi * 2 + (x & 1)) * NWORK + (w - 4) * 56 + (lane < 56 ? lane : 0)) * 64, base, 6);
                lds_arrive(LB + CNT(0));
                if (w == 4) {
                    lds_wait(L, CNT(0), 4);
                    st64(rs, lane == 0 ? O_RB + (x * 2 + hi) * 128 : OOB, basev);
                    FENCE();
                }
            }
        } else {
            if (w == 1 && hid < 21) {
                float a = 0.f;
                if (hid < 14) {
#pragma unroll
                    for (int i = 1; i < 8; ++i) a += *(LAS float*)(L + 73728 + i * 256 + lane * 4);
                    a = rbf(a);
                } else {
                    v2u pp[7];
#pragma unroll 1
                    for (int spins = 0;; ++spins) {
                        asm volatile("" ::: "memory");
                        unsigned d = 0;
#pragma unroll
                        for (int i = 0; i < 7; ++i) pp[i] = ld64(rs, O_MODP + (i * 1792 + (7 + hid) * 64 + lane) * 8);
#pragma unroll
                        for (int i = 0; i < 7; ++i) d |= pp[i].y ^ TAG32;
                        if (wall(d == 0)) break;
                        __builtin_amdgcn_s_sleep(6);
                        if (spins > SPIN_MAX) { __builtin_amdgcn_raw_buffer_store_b32(8, rs, O_TMO, 0, SC1); break; }
                    }
                    a = ((u2f(pp[0].x) + u2f(pp[1].x)) + (u2f(pp[2].x) + u2f(pp[3].x))) + ((u2f(pp[4].x) + u2f(pp[5].x)) + (u2f(pp[6].x) + 0.f));
                    a = rbf(a) + 1.f;
                }
                const int fs = hid < 7 ? hid : hid < 14 ? hid + 7 : hid - 7;
                st64(rs, (lane & 1) ? OOB : O_MODF + (fs * 32 + (lane >> 1)) * 8, v2u{pk(a, dppx<0xB1>(a)), TAG32});
                FENCE();
            }
            const int sq = tid & 7, pg = tid >> 3;
#pragma unroll 1
            for (int st = 0; st < 2; ++st) {
                const int sb = st * 32 + 4 * (4 * (x & 1) + (x >> 1)) + hi;
                int xo = O_X1P + sb * (NP * 128) + sq * 16 + pg * 128;
                PIN(xo);
                if (w == 0) {
                    wait_words<8>(rs, O_RB + (x * 2 + st) * 128, base, 9);
                    lds_arrive(LB + CNT(3 + st));
                } else lds_wait(L, CNT(3 + st), 1);
                v4u xv[7];
#pragma unroll
                for (int i = 0; i < 7; ++i) xv[i] = ld128(rs, xo + i * 4096);
                float s1[4], s2[4];
#pragma unroll
                for (int j = 0; j < 4; ++j) {
                    s1[j] = 0.f; s2[j] = 0.f;
#pragma unroll
                    for (int i = 0; i < 7; ++i) { s1[j] = dot2(xv[i][j], 0x3f803f80u, s1[j]); s2[j] = dot2(xv[i][j], xv[i][j], s2[j]); }
                }
                const int lo_ = st * 16384;
                *(LAS v4f*)(L + lo_ + pg * 144 + sq * 16) = v4f{s1[0], s1[1], s1[2], s1[3]};
                *(LAS v4f*)(L + lo_ + 4608 + pg * 144 + sq * 16) = v4f{s2[0], s2[1], s2[2], s2[3]};
                lds_arrive(LB + CNT(2));
                lds_wait(L, CNT(2), 4 * st + 4);
                const int rr = w * 8 + (lane >> 3), part = lane & 3, arr = (lane >> 2) & 1;
                float a = 0.f;
#pragma unroll
                for (int i = 0; i < 8; ++i) a += *(LAS float*)(L + lo_ + arr * 4608 + (part * 8 + i) * 144 + rr * 4);
                a += dppx<0xB1>(a); a += dppx<0x4E>(a);
                const float c = dppx<0x141>(a);
                const float md = a * (1.f / D);
                const float var = fmaxf(c * (1.f / D) - md * md, 0.f);
                const float sd = __builtin_amdgcn_sqrtf(rbf(rbf(var) + 1e-5f));
                st64(rs, (lane & 7) == 0 ? O_ST2 + (sb * 32 + rr) * 8 : OOB, v2u{pk(md, sd), TAG32});
                FENCE();
            }
            if (hid == 7 && tid == 0) {
                const u64 e = epoch + 1;
                st64(rs, O_EPOCH, v2u{(unsigned)e, (unsigned)(e >> 32)});
            }
        }
    } else {
        const int g = b >> 5, rt = b & 31, idx = b >> 3;
        const int rt2 = idx / 7, nt2 = idx - 7 * rt2;
        const int nt1 = 7 * (x >> 1) + nt2, pn1 = 4 * (x & 1) + rt2;
        const int cl = w >> 2;
        const int la_ = lane & 15, q_ = lane >> 4, c31 = lane & 31, h_ = lane >> 5;
        const int r8 = lane >> 3, ch = lane & 7, tr_e = la_ >> 2;
        const int trx = ((tr_e >> 1) & 1) * 64;
        const int sb1 = cl * 32 + 4 * pn1 + (w & 3);
        unsigned WD[14][8];
        v4u WF[14][2];
        const rsrc_t rwd = __builtin_amdgcn_make_buffer_rsrc((void*)p.wd, 0, F * D * 2, 0x00020000);
        const int wdo = ((224 * w + 4 * h_) * NP + nt2 * 32 + c31) * 4;
#define LDWD(kk2, j) __builtin_amdgcn_raw_buffer_load_b32(rwd, wdo + (kk2) * (16 * 896) + ((j) >> 2) * (8 * 896) + ((j) & 3) * 896, 0, 0)
        auto load_wd = [&](auto lo_c, auto hi_c) {
            constexpr int LO = decltype(lo_c)::value, HI = decltype(hi_c)::value;
#pragma unroll
            for (int kk2 = LO; kk2 < HI + WLAG; ++kk2) {
                if (kk2 < HI) {
#pragma unroll
                    for (int j = 0; j < 8; ++j) WD[kk2][j] = LDWD(kk2, j);
                }
                if (kk2 - WLAG >= LO) {
#pragma unroll
                    for (int i = 0; i < 4; ++i) {
                        WF[kk2 - WLAG][0][i] = __builtin_amdgcn_perm(WD[kk2 - WLAG][2 * i + 1], WD[kk2 - WLAG][2 * i], 0x05040100);
                        WF[kk2 - WLAG][1][i] = __builtin_amdgcn_perm(WD[kk2 - WLAG][2 * i + 1], WD[kk2 - WLAG][2 * i], 0x07060302);
                    }
                    asm volatile("" : "+v"(WF[kk2 - WLAG][0]), "+v"(WF[kk2 - WLAG][1]));
                }
                SCHED();
            }
        };
        if (w >= 6) {
            v4u xr[7];
            if (w == 7) {
#pragma unroll
                for (int i = 0; i < 8; ++i) glds16(p.ssmk + (size_t)(2 * b) * S + i * 512 + lane * 8, LB + L_KL + (i >> 2) * KST + 32 + (i & 3) * 1024);
                *(LAS v4u*)(L + L_U + h_ * UST + 4096 + c31 * 16) = v4u{0, 0, 0, 0};
                if (lane < 8) *(LAS v4u*)(L + (lane < 4 ? L_KL + (lane >> 1) * KST + (lane & 1) * 16 : L_MISC + (lane - 4) * 16)) = v4u{0, 0, 0, 0};
            } else {
                code_pf();
#pragma unroll
                for (int i = 0; i < 7; ++i) xr[i] = __builtin_amdgcn_raw_buffer_load_b128(rq, (8 * b + r8) * 896 + (ch + 8 * i) * 16, 0, AUXX);
            }
            SCHED();
            ISSUE_EPOCH();
            GET_EPOCH();
            BAR();
            FENCE();
            if (w == 7) { DRAIN(); lds_arrive(LB + CNT(1)); }
            else stats1(xr);
            FENCE();
            const u16* sm = (w & 1) ? p.wu : p.wg;
            unsigned sv0 = (r8 * F + nt1 * 64 + (ch ^ (((r8 >> 1) & 1) << 2)) * 8) * 2;
            PIN(sv0);
            const int s0 = 1024 * (7 - w) + 4 * lane;
            int kvo = O_KVP + b * (S * 4) + s0 * 4, ua = L_U + (2044 - s0 - 768) * 2;
            PIN(kvo); PIN(ua);
            lds_wait(L, CNT(0), 4);
            if (ADEL) __builtin_amdgcn_s_sleep(ADEL);
            FENCE();
            {
                v4u k0[4], k1[4];
#pragma unroll 1
                for (int spins = 0;; ++spins) {
                    asm volatile("" ::: "memory");
#pragma unroll
                    for (int i = 0; i < 4; ++i) { k0[i] = ld128(rs, kvo + i * 1024); k1[i] = ld128(rs, kvo + KVPL + i * 1024); }
                    const unsigned T = TAG32;
                    unsigned d = 0;
#pragma unroll
                    for (int i = 0; i < 4; ++i) d |= ((k0[i].y ^ T) | (k0[i].w ^ T)) | ((k1[i].y ^ T) | (k1[i].w ^ T));
                    PIN(d);
                    if (wall(d == 0)) break;
                    if (ASLP) __builtin_amdgcn_s_sleep(ASLP);
                    if (spins > SPIN_MAX) { __builtin_amdgcn_raw_buffer_store_b32(1, rs, O_TMO, 0, SC1); break; }
                }
                FENCE();
#pragma unroll
                for (int i = 0; i < 4; ++i) {
                    *(LAS v2u*)(L + ua + (3 - i) * 512) = v2u{__builtin_amdgcn_perm(k1[i].x, k1[i].z, 0x05040100), __builtin_amdgcn_perm(k0[i].x, k0[i].z, 0x05040100)};
                    *(LAS v2u*)(L + ua + (3 - i) * 512 + UST) = v2u{__builtin_amdgcn_perm(k1[i].x, k1[i].z, 0x07060302), __builtin_amdgcn_perm(k0[i].x, k0[i].z, 0x07060302)};
                }
            }
            lds_arrive(LB + CNT(w));
            FENCE();
            lds_wait(L, CNT(13 - w), 1);
            lds_wait(L, CNT(2), 2);
#pragma unroll 1
            for (int qi = 0; qi < 4; ++qi) {
                const int k0 = 7 * ((rt2 + qi) & 3);
                w1_unit<63, 7>(sm, sv0 + k0 * 16 * F * 2, LB + L_W1 + (w & 1) * 2048 + k0 * 4096);
            }
            FENCE();
            {
                const int mfo = O_MODF + (w - 6) * 2048 + lane * 16, mfo2 = (w == 6 || lane < 32) ? mfo + 1024 : O_MODF + (14 + nt2) * 256 + la_ * 16;
                v4u f0, f1;
#pragma unroll 1
                for (int spins = 0;; ++spins) {
                    asm volatile("" ::: "memory");
                    f0 = ld128(rs, mfo); f1 = ld128(rs, mfo2);
                    const unsigned T = TAG32;
                    if (__builtin_expect(wall((((f0.y ^ T) | (f0.w ^ T)) | ((f1.y ^ T) | (f1.w ^ T))) == 0), 1)) break;
                    if (spins > SPIN_MAX) { __builtin_amdgcn_raw_buffer_store_b32(7, rs, O_TMO, 0, SC1); break; }
                }
                const int co = L_CS + (w - 6) * 1024 + lane * 8;
                *(LAS v2u*)(L + co) = v2u{f0.x, f0.z};
                *(LAS v2u*)(L + ((w == 7 && lane >= 32) ? L_GB + la_ * 8 : co + 512)) = v2u{f1.x, f1.z};
            }
            DRAIN();
            lds_arrive(LB + CNT(13));
            FENCE();
            lds_wait(L, CNT(13), 2);
            const int mo = L_CS + ((lane & 1) ? 0 : 896) + 16 * q_;
            const int uo = L_W1 + (q_ >> 1) * 4096 + (w & 1) * 2048 + (8 * (q_ & 1) + tr_e) * 128 + 8 * (lane & 3);
            v4f uc[4];
#pragma unroll
            for (int i = 0; i < 4; ++i) uc[i] = v4f{0.f, 0.f, 0.f, 0.f};
#pragma unroll 2
            for (int k0 = 0; k0 < 14; ++k0) {
                const v4u xf = *(LAS v4u*)(L + mo + k0 * 64);
#pragma unroll
                for (int i = 0; i < 4; ++i) uc[i] = mfma16(xf, trfrag(L, uo + k0 * 8192 + (((i >> 1) * 64) ^ trx) + (i & 1) * 32), uc[i]);
            }
            const float u1 = q_ == 0 ? uc[0][0] : q_ == 1 ? uc[1][0] : q_ == 2 ? uc[2][0] : uc[3][0];
            const float u2 = q_ == 0 ? uc[0][1] : q_ == 1 ? uc[1][1] : q_ == 2 ? uc[2][1] : uc[3][1];
            const unsigned h1 = pk(u1, u2);
            const float r0 = u1 - lo2f(h1), r1 = u2 - hi2f(h1);
            const unsigned h2 = pk(r0, r1);
            const unsigned h3 = pk(r0 - lo2f(h2), r1 - hi2f(h2));
            *(LAS v4u*)(L + L_UV + (w - 6) * 1024 + lane * 16) =
                v4u{__builtin_amdgcn_perm(h2, h1, 0x05040100), __builtin_amdgcn_perm(h1, h3, 0x07060100), __builtin_amdgcn_perm(h3, h2, 0x07060302), 0u};
            lds_arrive(LB + CNT(5));
            FENCE();
        } else if (w >= 4) {
            if (w == 5) {
                v4u wm[8]; unsigned cv;
                const int us = b / 7, ks = b - 7 * us;
                mod_load(wm, cv, us < 21 ? us : us + 7, ks, b < 196);
                SCHED();
                ISSUE_EPOCH();
                GET_EPOCH();
                asm volatile("s_barrier" ::: "memory");
                FENCE();
                const float mu = mod_unit(wm, cv, L_MS);
                st64(rs, b < 196 ? O_MODP + (ks * 1792 + us * 64 + lane) * 8 : OOB, v2u{f2u(mu), TAG32});
                lds_arrive(LB + CNT(2));
                FENCE();
            } else {
                unsigned xx[32];
                const int o0 = ((64 * rt + 32 * h_) * NP + 32 * g + c31) * 4;
#pragma unroll
                for (int i = 0; i < 32; ++i) xx[i] = __builtin_amdgcn_raw_buffer_load_b32(rq, o0 + i * 896, 0, AUXX);
                SCHED();
                ISSUE_EPOCH();
                GET_EPOCH();
                asm volatile("s_barrier" ::: "memory");
                int wa = L_XT + c31 * 256, ra = L_XT + q_ * 256, go = O_XP + (32 * g + q_) * (S * 4) + (64 * rt + 4 * la_) * 4;
                PIN(wa); PIN(ra); PIN(go);
                SCHED();
#pragma unroll
                for (int i = 0; i < 8; ++i)
                    *(LAS v4u*)(L + wa + (((8 * h_ + i) ^ la_) * 16)) = v4u{xx[4 * i], xx[4 * i + 1], xx[4 * i + 2], xx[4 * i + 3]};
                FENCE();
                const unsigned T = TAG32;
#pragma unroll
                for (int i = 0; i < 8; ++i) {
                    v4u a = *(LAS v4u*)(L + ra + i * 1024 + ((la_ ^ ((4 * i + q_) & 15)) * 16));
                    st128(rs, go + i * 4 * (S * 4), v4u{a.x, T, a.y, T});
                    st128(rs, go + i * 4 * (S * 4) + KVPL, v4u{a.z, T, a.w, T});
                }
                lds_arrive(LB + CNT(2));
                FENCE();
            }
            const int t0 = 2 * (w - 4);
            int xpo = O_XP + b * (S * 4) + (256 * t0 + 4 * lane) * 4, sto = O_ST1 + (256 * t0 + 4 * lane) * 8;
            int mpo = O_MODP + ((ch < 7 ? ch : 0) * 1792 + q_ * D + 2 * b + (r8 & 1)) * 8;
            int exr = L_EX + t0 * 1024 + (lane ^ (r8 & 3)) * 16;
            int apo = O_AP + ((8 * t0 + r8) * NP + b) * 128 + ch * 16;
            int cbo = lane == 0 ? O_FB + ((w - 4) * NWORK + b) * 64 : OOB - 2 * NWORK * 64;
            PIN(xpo); PIN(sto); PIN(mpo); PIN(exr); PIN(apo); PIN(cbo);
            FENCE();
            __builtin_amdgcn_s_setprio(3);
            if (XDEL) __builtin_amdgcn_s_sleep(XDEL);
            v4u xa4[2], xb4[2], sA[2], sB[2]; v2u mp;
            v4u x4[2];
            float sa0, sa1, ca0, ca1, ga0, ga1, cb0, cb1;
            float c1[2][8];
            auto ld_tag = [&](int st) {
#pragma unroll
                for (int i = 0; i < 2; ++i) {
                    xa4[i] = ld128(rs, xpo + st * 4096 + i * 1024); xb4[i] = ld128(rs, xpo + KVPL + st * 4096 + i * 1024);
                    sA[i] = ld128(rs, sto + st * 8192 + i * 2048); sB[i] = ld128(rs, sto + st * 8192 + i * 2048 + 16);
                }
            };
#pragma unroll 1
            for (int spins = 0;; ++spins) {
                asm volatile("" ::: "memory");
                mp = ld64(rs, mpo);
                if (wall(mp.y == TAG32)) break;
                __builtin_amdgcn_s_sleep(XSLP);
                if (spins > SPIN_MAX) { __builtin_amdgcn_raw_buffer_store_b32(14, rs, O_TMO, 0, SC1); break; }
            }
            FENCE();
            ld_tag(0);
            SCHED();
            {
                float mv = ch < 7 ? u2f(mp.x) : 0.f;
                mv += dppx<0xB1>(mv); mv += dppx<0x4E>(mv); mv += dppx<0x141>(mv);
                mv = rbf(rbf(mv) + ((q_ & 1) ? 1.f : 0.f));
                sa0 = rdl(mv, 0); sa1 = rdl(mv, 8); ca0 = rdl(mv, 16); ca1 = rdl(mv, 24); ga0 = rdl(mv, 32); ga1 = rdl(mv, 40); cb0 = rdl(mv, 48); cb1 = rdl(mv, 56);
            }
#pragma unroll
            for (int st = 0; st < 2; ++st) {
#pragma unroll 1
                for (int spins = 0;; ++spins) {
                    const unsigned T = TAG32;
                    unsigned d = 0u;
#pragma unroll
                    for (int i = 0; i < 2; ++i)
                        d |= (((sA[i].y ^ T) | (sA[i].w ^ T)) | ((sB[i].y ^ T) | (sB[i].w ^ T))) | (((xa4[i].y ^ T) | (xa4[i].w ^ T)) | ((xb4[i].y ^ T) | (xb4[i].w ^ T)));
                    PIN(d);
                    if (wall(d == 0)) break;
                    __builtin_amdgcn_s_sleep(XSLP);
                    asm volatile("" ::: "memory");
                    ld_tag(st);
                    if (spins > SPIN_MAX) { __builtin_amdgcn_raw_buffer_store_b32(2, rs, O_TMO, 0, SC1); break; }
                }
                if (st == 0) FENCE();
#pragma unroll
                for (int i = 0; i < 2; ++i) x4[i] = v4u{xa4[i].x, xa4[i].z, xb4[i].x, xb4[i].z};
#pragma unroll
                for (int tt = 0; tt < 2; ++tt) {
                    const unsigned st1[4] = {sA[tt].x, sA[tt].z, sB[tt].x, sB[tt].z};
#pragma unroll
                    for (int i = 0; i < 4; ++i) {
                        const float xa = lo2f(x4[tt][i]), xb = hi2f(x4[tt][i]);
                        const float mean = lo2f(st1[i]), rstd = __builtin_amdgcn_rcpf(hi2f(st1[i]));
                        const float n0 = fmaf((xa - mean) * rstd, ca0, sa0), n1 = fmaf((xb - mean) * rstd, ca1, sa1);
                        c1[tt][2 * i] = n0 * ga0; c1[tt][2 * i + 1] = n1 * ga1;
                        PIN(c1[tt][2 * i]); PIN(c1[tt][2 * i + 1]);
                    }
                }
                if (st == 0) { SCHED(); ld_tag(1); FENCE(); }
                if (st == 1) { DRAIN(); st64(rs, cbo, basev); lds_arrive(LB + CNT(12)); FENCE(); }
                lds_wait(L, CNT(14 + st), 4);
                if (st == 0) FENCE();
#pragma unroll
                for (int tt = 0; tt < 2; ++tt) {
                    const int eo = exr + st * 16384 + tt * 1024;
                    const v4f y0 = *(LAS v4f*)(L + eo) + *(LAS v4f*)(L + eo + 4096), y1 = *(LAS v4f*)(L + eo + 8192) + *(LAS v4f*)(L + eo + 12288);
                    unsigned oa[4], ox[4];
#pragma unroll
                    for (int i = 0; i < 4; ++i) {
                        ox[i] = pk(fmaf(c1[tt][2 * i], y0[i], lo2f(x4[tt][i])), fmaf(c1[tt][2 * i + 1], y1[i], hi2f(x4[tt][i])));
                        oa[i] = pk(lo2f(ox[i]) * cb0, hi2f(ox[i]) * cb1);
                    }
                    const int go = apo + (st * 32 + tt * 8) * NP * 128;
                    st128(rs, go, v4u{oa[0], oa[1], oa[2], oa[3]});
                    st128(rs, go + (O_X1P - O_AP), v4u{ox[0], ox[1], ox[2], ox[3]});
                }
                if (st) lds_arrive(LB + CNT(11));
                if (st == 1) { DRAIN(); st64(rs, cbo + 2 * NWORK * 64, basev); }
            }
            __builtin_amdgcn_s_setprio(0);
            FENCE();
        } else {
            {
                unsigned kk[11], vv[8];
                const int rg = tid >> 5;
                const int row0 = 64 * rt + 8 * rg, col = 32 * g + c31;
                const rsrc_t rk = __builtin_amdgcn_make_buffer_rsrc((void*)p.k, 0, S * D * 2, 0x00020000);
                const rsrc_t rv = __builtin_amdgcn_make_buffer_rsrc((void*)p.v, 0, S * D * 2, 0x00020000);
                const int o0 = (row0 * NP + col) * 4;
                const int oh = row0 ? o0 - 3 * 896 : 0x40000000;
                const v4u sh = __builtin_amdgcn_raw_buffer_load_b128(__builtin_amdgcn_make_buffer_rsrc((void*)p.shk, 0, D * 8, 0x00020000), col * 16, 0, 0);
                SCHED();
#pragma unroll
                for (int i = 0; i < 3; ++i) kk[i] = __builtin_amdgcn_raw_buffer_load_b32(rk, oh + i * 896, 0, AUXIN);
#pragma unroll
                for (int i = 0; i < 8; ++i) kk[3 + i] = __builtin_amdgcn_raw_buffer_load_b32(rk, o0 + i * 896, 0, AUXIN);
#pragma unroll
                for (int i = 0; i < 8; ++i) vv[i] = __builtin_amdgcn_raw_buffer_load_b32(rv, o0 + i * 896, 0, AUXIN);
                SCHED();
                ISSUE_EPOCH();
                const int p2 = 8 * w + q_;
                int wa = L_EX + c31 * 256 + ((2 * rg) ^ la_) * 16;
                int ra0 = L_EX + p2 * 256 + ((la_ ^ (p2 & 15)) * 16), ra1 = L_EX + (p2 + 4) * 256 + ((la_ ^ ((p2 + 4) & 15)) * 16);
                int go = O_KVP + (32 * g + p2) * (S * 4) + (64 * rt + 4 * la_) * 4, go1 = go + 4 * (S * 4);
                PIN(wa); PIN(ra0); PIN(ra1); PIN(go); PIN(go1);
                GET_EPOCH();
                SCHED();
                float s0[4] = {lo2f(sh.x), hi2f(sh.x), lo2f(sh.y), hi2f(sh.y)};
                float s1[4] = {lo2f(sh.z), hi2f(sh.z), lo2f(sh.w), hi2f(sh.w)};
                unsigned o[8];
#pragma unroll
                for (int i = 0; i < 8; ++i) {
                    float a0, a1;
                    a0 = lo2f(kk[i]) * s0[3]; a1 = hi2f(kk[i]) * s1[3]; rb2(a0, a1);
#pragma unroll
                    for (int t = 1; t < 4; ++t) {
                        float m0 = lo2f(kk[i + t]) * s0[3 - t], m1 = hi2f(kk[i + t]) * s1[3 - t]; rb2(m0, m1);
                        a0 += m0; a1 += m1; rb2(a0, a1);
                    }
                    o[i] = pk(a0 * lo2f(vv[i]), a1 * hi2f(vv[i]));
                }
                *(LAS v4u*)(L + wa) = v4u{o[0], o[1], o[2], o[3]};
                *(LAS v4u*)(L + (wa ^ 16)) = v4u{o[4], o[5], o[6], o[7]};
                BAR();
                FENCE();
                const v4u a = *(LAS v4u*)(L + ra0), c = *(LAS v4u*)(L + ra1);
                const unsigned T = TAG32;
                st128(rs, go, v4u{a.x, T, a.y, T});
                st128(rs, go + KVPL, v4u{a.z, T, a.w, T});
                st128(rs, go1, v4u{c.x, T, c.y, T});
                st128(rs, go1 + KVPL, v4u{c.z, T, c.w, T});
                lds_arrive(LB + CNT(0));
                FENCE();
            }
            const int cc = w >> 1, i0 = 4 * (w & 1);
            lds_wait(L, CNT(1), 1);
            v4u KF[4][8];
            int kb = L_KL + cc * KST + ((2 + 2 * la_ + 16 * q_) & ~3) + 64 * i0;
            const int shf = (la_ & 1) ? 0 : 16;
#pragma unroll
            for (int i2 = 0; i2 < 2; ++i2) {
                unsigned d[8][5];
#pragma unroll
                for (int n = 0; n < 8; ++n)
#pragma unroll
                    for (int j = 0; j < 5; ++j) d[n][j] = *(LAS unsigned*)(L + kb + (2 * i2 + (n >> 2)) * 64 + (n & 3) * 512 + j * 4);
                SCHED();
#pragma unroll
                for (int n = 0; n < 8; ++n) {
#pragma unroll
                    for (int j = 0; j < 4; ++j) KF[2 * i2 + (n >> 2)][n & 3][j] = __builtin_amdgcn_alignbit(d[n][j + 1], d[n][j], shf);
                    PIN(KF[2 * i2 + (n >> 2)][n & 3]);
                }
            }
            v4u KD;
            {
                const int j = 15 - la_ - 8 * q_;
                const unsigned dv = (w & 1) ? 0u : ((cc ? m_D >> 16 : m_D & 0xffffu) << ((j & 1) * 16));
#pragma unroll
                for (int i = 0; i < 4; ++i) KD[i] = (j >> 1) == i ? dv : 0u;
            }
            int ub = L_U + cc * UST + (2032 - 16 * la_ + 8 * q_ + 32 * i0) * 2 - 3584;
            int exw = L_EX + w * 4096 + (4 * la_ + (q_ ^ ((la_ >> 1) & 3))) * 16;
            PIN(KD); PIN(ub); PIN(exw);
            lds_wait(L, CNT(0), 4);
            FENCE();
            lds_wait(L, CNT(7), 1);
            FENCE();
            v4f acc[8];
#pragma unroll
            for (int t = 0; t < 8; ++t) acc[t] = v4f{0.f, 0.f, 0.f, 0.f};
            v4u UF[4][4];
#pragma unroll
            for (int ii = 0; ii < 4; ++ii)
#pragma unroll
                for (int r = 0; r < 4; ++r) UF[ii][r] = *(LAS v4u*)(L + ub + 3584 + ii * 64 - r * 512);
            SCHED();
#pragma unroll
            for (int ii = 0; ii < 4; ++ii)
#pragma unroll
                for (int r = 0; r < 4; ++r) {
                    if (ii == 0) acc[r] = mfma16(KD, UF[0][r], acc[r]);
#pragma unroll
                    for (int sg = 0; sg + r < 4; ++sg) acc[sg + r] = mfma16(KF[ii][sg], UF[ii][r], acc[sg + r]);
                }
            SCHED();
#pragma unroll
            for (int r = 1; r < 4; ++r)
#pragma unroll
                for (int sg = 4 - r; sg < 4; ++sg) acc[sg + r] = mfma16(KF[0][sg], UF[0][r], acc[sg + r]);
            SCHED();
#pragma unroll
            for (int t = 0; t < 4; ++t) *(LAS v4f*)(L + exw + t * 1024) = acc[t];
            lds_arrive(LB + CNT(14));
            FENCE();
            lds_wait(L, CNT(6), 1);
#pragma unroll
            for (int ii = 0; ii < 4; ++ii) {
                v4u UG[4];
                unsigned d[4][5];
#pragma unroll
                for (int r = 0; r < 4; ++r) UG[r] = *(LAS v4u*)(L + ub + 1536 + ii * 64 - r * 512);
#pragma unroll
                for (int n = 0; n < 4; ++n)
#pragma unroll
                    for (int j = 0; j < 5; ++j) d[n][j] = *(LAS unsigned*)(L + kb + ii * 64 + (4 + n) * 512 + j * 4);
                SCHED();
                if (ii) {
#pragma unroll
                    for (int r = 1; r < 4; ++r)
#pragma unroll
                        for (int sg = 4 - r; sg < 4; ++sg) acc[sg + r] = mfma16(KF[ii][sg], UF[ii][r], acc[sg + r]);
                }
                SCHED();
#pragma unroll
                for (int r = 0; r < 4; ++r) {
                    if (ii == 0) acc[4 + r] = mfma16(KD, UG[r], acc[4 + r]);
#pragma unroll
                    for (int sg = 0; sg + r < 4; ++sg) acc[4 + sg + r] = mfma16(KF[ii][sg], UG[r], acc[4 + sg + r]);
                }
#pragma unroll
                for (int n = 0; n < 4; ++n)
#pragma unroll
                    for (int j = 0; j < 4; ++j) KF[ii][4 + n][j] = __builtin_amdgcn_alignbit(d[n][j + 1], d[n][j], shf);
#pragma unroll
                for (int t = 0; t < 8; ++t) { __builtin_amdgcn_sched_group_barrier(0x8, 1, 0); __builtin_amdgcn_sched_group_barrier(0x2, 2, 0); }
                SCHED();
#pragma unroll
                for (int r = 0; r < 4; ++r)
#pragma unroll
                    for (int sg = 4; sg + r < 8; ++sg) acc[sg + r] = mfma16(KF[ii][sg], UF[ii][r], acc[sg + r]);
                SCHED();
            }
            lds_arrive(LB + CNT(1));
            lds_wait(L, CNT(1), 5);
#pragma unroll
            for (int t = 0; t < 4; ++t) *(LAS v4f*)(L + exw + 16384 + t * 1024) = acc[4 + t];
            lds_arrive(LB + CNT(15));
            FENCE();
        }
        if (!cl) lds_wait(L, CNT(12), 2);
        load_wd(IC<0>{}, IC<SWD>{});
        if (SWD < 14 && cl) load_wd(IC<SWD>{}, IC<14>{});
        FENCE();
        const int trb = L_W1 + (8 * h_ + tr_e) * 128 + 32 * (q_ & 1) + 8 * (lane & 3);
        int tb[4] = {trb + trx, trb + (64 ^ trx), trb + trx + 57344, trb + (64 ^ trx) + 57344};
        PIN(tb[0]); PIN(tb[1]); PIN(tb[2]); PIN(tb[3]);
#define FK(f) ((f) < 80 ? (f) >> 2 : 20 + (((f) - 80) & 15) / 2)
#define FT(f) ((f) < 80 ? (f) & 3 : (((f) - 80) >> 4) + 2 * ((f) & 1))
        auto wfrag = [&](int f) -> v4u {
            const int k1 = FK(f), tile = FT(f);
            return trfrag(L, tb[(k1 >= 14 ? 2 : 0) + (tile & 1)] + (k1 >= 14 ? k1 - 14 : k1) * 4096 + (tile >> 1) * 2048);
        };
        int xo = O_AP + sb1 * (NP * 128) + h_ * 512 + c31 * 4;
        PIN(xo);
        if ((w & 3) == 0) { wait_words<BSLP>(rs, O_RB + (x * 2 + cl) * 128, base, 3); lds_arrive(LB + CNT(3 + cl)); }
        else lds_wait(L, CNT(3 + cl), 1);
        FENCE();
        constexpr int WIN = XWIN;
        v4u XF[WIN];
        auto ldx = [&](int k1) -> v4u {
            return v4u{ld32(rs, xo + k1 * 1024), ld32(rs, xo + k1 * 1024 + 128), ld32(rs, xo + k1 * 1024 + 256), ld32(rs, xo + k1 * 1024 + 384)};
        };
#pragma unroll
        for (int i = 0; i < WIN; ++i) XF[i] = ldx(i);
        SCHED();
        lds_wait(L, CNT(5), 2);
        if (cl) __builtin_amdgcn_s_setprio(1); else __builtin_amdgcn_s_setprio(2);
        v16f ga[4];
#pragma unroll
        for (int t = 0; t < 4; ++t)
#pragma unroll
            for (int i = 0; i < 16; ++i) ga[t][i] = 0.f;
        v2u st2, fca;
        const int fco = O_FC + ((4 * x + rt2) * 28 + ((7 * w) >> 1) + (lane & 3)) * 64;
        const int gbase = O_G + ((4 * x + rt2) * 112 + 14 * w) * 1024 + lane * 16;
        v4u GF[14];
        {
            static_assert(WIN == 8, "the last eight X fragments stay resident");
            v4u WA[RD];
#pragma unroll
            for (int f = 0; f < RD - 1; ++f) WA[f] = wfrag(f);
            SCHED();
            auto step = [&](int f) {
                ga[FT(f)] = mfma32(WA[f % RD], XF[FK(f) % WIN], ga[FT(f)]);
                if (f + RD - 1 < 112) WA[(f + RD - 1) % RD] = wfrag(f + RD - 1);
            };
#pragma unroll
            for (int k1 = 0; k1 < 24; ++k1) {
#pragma unroll
                for (int t = 0; t < 4; ++t) step(4 * k1 + t);
                if (k1 >= 1 && k1 - 1 + WIN < 28) XF[(k1 - 1) % WIN] = ldx(k1 - 1 + WIN);
                if (k1 == 16) st2 = ld64(rs, O_ST2 + (sb1 * 32 + c31) * 8);
#pragma unroll
                for (int t = 0; t < 4; ++t) {
                    __builtin_amdgcn_sched_group_barrier(0x8, 1, 0);
                    __builtin_amdgcn_sched_group_barrier(0x100, 2, 0);
                    __builtin_amdgcn_sched_group_barrier(0x2, 1, 0);
                    __builtin_amdgcn_sched_group_barrier(0x20, 1, 0);
                }
                SCHED();
                if (k1 == 0) FENCE();
                if (k1 == 22) {
                    if (__builtin_expect(!wall(st2.y == TAG32), 0)) {
#pragma unroll 1
                        for (int spins = 0;; ++spins) {
                            asm volatile("" ::: "memory");
                            st2 = ld64(rs, O_ST2 + (sb1 * 32 + c31) * 8);
                            if (wall(st2.y == TAG32)) break;
                            if (spins > SPIN_MAX) { __builtin_amdgcn_raw_buffer_store_b32(4, rs, O_TMO, 0, SC1); break; }
                        }
                    }
                    SCHED();
                }
            }
            FENCE();
            const unsigned sz = h_ ? 0u : st2.x ^ 0x8000u;
            const v4u xe4 = {__builtin_amdgcn_perm(sz, sz, 0x01000100), sz, __builtin_amdgcn_perm(sz, sz, 0x03020302), 0u};
            const float sd = hi2f(st2.x), sd2 = sd * sd, ce = -1.44269504088896f * __builtin_amdgcn_rcpf(sd);
            const int go = O_G + (sb1 * 112 + 4 * nt1) * 1024 + lane * 16;
            auto epi = [&](int t) {
                unsigned o[4];
#pragma unroll
                for (int i = 0; i < 4; ++i) {
                    const int r = (t & 1) * 8 + 2 * i;
                    const float g0 = ga[t >> 1][r], g1 = ga[t >> 1][r + 1];
                    const float e0 = __builtin_amdgcn_exp2f(g0 * ce), e1 = __builtin_amdgcn_exp2f(g1 * ce);
                    const float q0 = __builtin_amdgcn_rcpf(fmaf(e0, sd2, sd2)), q1 = __builtin_amdgcn_rcpf(fmaf(e1, sd2, sd2));
                    o[i] = pk(g0 * ga[2 + (t >> 1)][r] * q0, g1 * ga[2 + (t >> 1)][r + 1] * q1);
                }
                st128(rs, go + t * 1024, v4u{o[0], o[1], o[2], o[3]});
            };
            ga[0] = mfma32(*(LAS v4u*)(L + L_UV + c31 * 16), xe4, ga[0]);
            ga[2] = mfma32(*(LAS v4u*)(L + L_UV + 1024 + c31 * 16), xe4, ga[2]);
            ga[1] = mfma32(*(LAS v4u*)(L + L_UV + 512 + c31 * 16), xe4, ga[1]);
            ga[3] = mfma32(*(LAS v4u*)(L + L_UV + 1536 + c31 * 16), xe4, ga[3]);
            SCHED();
#pragma unroll
            for (int hf = 0; hf < 2; ++hf) {
#pragma unroll
                for (int f = 96 + 8 * hf; f < 104 + 8 * hf; ++f) step(f);
                epi(hf);
#pragma unroll
                for (int t = 0; t < 8; ++t) {
                    __builtin_amdgcn_sched_group_barrier(0x8, 1, 0);
                    __builtin_amdgcn_sched_group_barrier(0x100, 2, 0);
                    __builtin_amdgcn_sched_group_barrier(0x402, 7, 0);
                }
                SCHED();
            }
            lds_arrive(LB + CNT(8));
            FENCE();
            __builtin_amdgcn_s_setprio(3);
            epi(2);
            fca = ld64(rs, fco);
            SCHED();
            epi(3);
            SCHED();
            __builtin_amdgcn_s_setprio(0);
            FENCE();
            DRAIN();
            st64(rs, lane == 0 ? O_FC + (sb1 * 28 + nt1) * 64 : OOB, basev);
        }
        FENCE();
        if (SWD < 14 && !cl) load_wd(IC<SWD>{}, IC<14>{});
#pragma unroll
        for (int st = 0; st < 2; ++st) {
            const int sb2 = st * 32 + 4 * x + rt2;
            int go = gbase + st * (32 * 112 * 1024);
            int x1g = O_X1P + (sb2 * NP + 32 * nt2 + 4 * w) * 128 + lane * 4;
            PIN(go); PIN(x1g);
            if (!wall(((fca.x ^ (unsigned)base) | (fca.y ^ (unsigned)(base >> 32))) == 0)) wait_words<CSLP>(rs, fco + st * (32 * 28 * 64), base, 5);
            __builtin_amdgcn_fence(__ATOMIC_ACQUIRE, "wavefront");
            asm volatile("" ::: "memory");
            FENCE();
#pragma unroll
            for (int i = 0; i < GW; ++i) GF[i] = ld128(rs, go + i * 1024);
            unsigned xa, xb;
            SCHED();
            v16f oa[2];
#pragma unroll
            for (int i = 0; i < 16; ++i) { oa[0][i] = 0.f; oa[1][i] = 0.f; }
#pragma unroll
            for (int kk2 = 0; kk2 < 14; ++kk2) {
                oa[0] = mfma32(WF[kk2][0], GF[kk2], oa[0]);
                oa[1] = mfma32(WF[kk2][1], GF[kk2], oa[1]);
                if (kk2 + GW < 14) GF[kk2 + GW] = ld128(rs, go + (kk2 + GW) * 1024);
                if (kk2 + GW == 14) { xa = ld32(rs, x1g); xb = ld32(rs, x1g + 256); }
                SCHED();
            }
            FENCE();
            if (st == 0) fca = ld64(rs, fco + 32 * 28 * 64);
            SCHED();
            const int po = (st ? 32768 + w * 8192 : (w < 4 ? L_EX + w * 8192 : (w - 4) * 8192)) + c31 * 128;
            if (st == 1 || w >= 4) lds_wait(L, CNT(8), 8); else lds_wait(L, CNT(11), 2);
#pragma unroll
            for (int t = 0; t < 2; ++t)
#pragma unroll
                for (int g4 = 0; g4 < 4; ++g4)
                    *(LAS v4f*)(L + po + t * 4096 + (((2 * g4 + h_) ^ (c31 & 7)) * 16)) =
                        v4f{oa[t][4 * g4], oa[t][4 * g4 + 1], oa[t][4 * g4 + 2], oa[t][4 * g4 + 3]};
            const int xw = (st ? L_X1B : L_X1A) + c31 * 144 + (4 * w + h_) * 4;
            *(LAS unsigned*)(L + xw) = xa; *(LAS unsigned*)(L + xw + 8) = xb;
            lds_arrive(LB + CNT(st ? 6 : 9));
            FENCE();
        }
        {
            const int sl = (tid >> 3) & 31, cg = tid & 7;
            const int ro = sl * 128 + ((cg ^ (sl & 7)) * 16);
            const int r1o = (cl ? 32768 : L_EX) + ro, r2o = (cl ? 65536 : 0) + ro;
            const int op = (((cl * 32 + 4 * x + rt2) * 32 + sl) * D + 64 * nt2 + 8 * cg) * 2;
            const v4u gbv = *(LAS v4u*)(L + L_GB + cg * 16);
            lds_wait(L, cl ? CNT(6) : CNT(9), cl ? 9 : 8);
            const v4u x1v = *(LAS v4u*)(L + (cl ? L_X1B : L_X1A) + sl * 144 + cg * 16);
            v4f r0 = *(LAS v4f*)(L + r1o), r1 = *(LAS v4f*)(L + r1o + 4096), r2 = *(LAS v4f*)(L + r2o), r3 = *(LAS v4f*)(L + r2o + 4096);
#pragma unroll
            for (int ww = 1; ww < 4; ++ww) {
                r0 += *(LAS v4f*)(L + r1o + ww * 8192); r1 += *(LAS v4f*)(L + r1o + ww * 8192 + 4096);
                r2 += *(LAS v4f*)(L + r2o + ww * 8192); r3 += *(LAS v4f*)(L + r2o + ww * 8192 + 4096);
            }
            r0 += r2; r1 += r3;
            unsigned o[4];
#pragma unroll
            for (int i = 0; i < 4; ++i) {
                o[i] = pk(fmaf(r0[i], lo2f(gbv[i]), lo2f(x1v[i])), fmaf(r1[i], hi2f(gbv[i]), hi2f(x1v[i])));
            }
            __builtin_amdgcn_raw_buffer_store_b128(v4u{o[0], o[1], o[2], o[3]}, __builtin_amdgcn_make_buffer_rsrc(p.out, 0, S * D * 2, 0x00020000), op, 0, SC1);
        }
        FENCE();
    }
}

#if defined(__HIP_DEVICE_COMPILE__)
#pragma clang attribute pop
#endif
extern "C" void kernel_launch(void* const* d_in, const int* in_sizes, int n_in,
                              void* d_out, int out_size, void* d_ws, size_t ws_size,
                              hipStream_t stream) {
    Params p;
    p.q = (const u16*)d_in[0]; p.k = (const u16*)d_in[1]; p.v = (const u16*)d_in[2];
    p.shk = (const u16*)d_in[3]; p.ssmk = (const u16*)d_in[4]; p.ssmD = (const u16*)d_in[5];
    p.wg = (const u16*)d_in[7]; p.wu = (const u16*)d_in[8]; p.wd = (const u16*)d_in[9];
    p.c = (const u16*)d_in[10]; p.wmod = (const u16*)d_in[11];
    p.out = (u16*)d_out; p.ws = (char*)d_ws;
    (void)hipFuncSetAttribute((const void*)fused, hipFuncAttributeMaxDynamicSharedMemorySize, L_TOTAL);
    fused<<<NWORK + NHELP, 512, L_TOTAL, stream>>>(p);
}
```
